```python
import torch

def _softmax(x, axis=-1):
    return torch.softmax(x, dim=axis)
def _gelu(x):
    return torch.nn.functional.gelu(x, approximate="tanh")
def _silu(x):
    return torch.nn.functional.silu(x)
def _sigmoid(x):
    return torch.sigmoid(x)
def _layernorm(x, axis=-1, eps=1e-5):
    m = x.mean(dim=axis, keepdim=True)
    v = x.var(dim=axis, keepdim=True, unbiased=False)
    return (x - m) / torch.sqrt(v + eps)
def _scatter_add(tgt, idx, src, axis=0):
    return tgt.clone().index_add_(axis, idx, src)
def _segment_sum(src, seg, n):
    out = torch.zeros((n,) + src.shape[1:], dtype=src.dtype)
    return out.index_add_(0, seg, src)
def _pad(x, pw):
    flat = tuple(v for pair in reversed(pw) for v in pair)
    return torch.nn.functional.pad(x, flat)

kernel_name = 'standalone__kernel_systems_refs_featgraph__graph'

def setup_inputs():

    _w = (lambda a: (lambda t: t.to(torch.bfloat16) if t.is_floating_point() else t)(torch.as_tensor(a)))
    N, deg, D = 25000, 16, 128
    E = N * deg
    scale = D ** -0.5
    src = (lambda lo, hi, *s: torch.randint(lo, hi, s))(0, N, E).to(torch.int64)
    dst = (lambda lo, hi, *s: torch.randint(lo, hi, s))(0, N, E).to(torch.int64)
    return {
        "x": _w((lambda *s: torch.randn(*s, dtype=torch.bfloat16) * 0.8)(N, D)),
        "src": _w(src),
        "dst": _w(dst),
        "w_q": _w((lambda *s: torch.randn(*s, dtype=torch.bfloat16) * 0.8)(D, D) * scale),
        "w_v": _w((lambda *s: torch.randn(*s, dtype=torch.bfloat16) * 0.05)(D, D) * scale),
    }

def reference(x, src, dst, w_q, w_v):
    N, D = x.shape
    xs = x[src]
    xd = x[dst]
    q = xd @ w_q
    logits = torch.einsum('ed,ed->e', xs, q) * (float(D) ** -0.5)
    alpha = _sigmoid(logits)
    msg = alpha[:, None] * (xs @ w_v)
    return _segment_sum(msg, dst, N)
```

```cpp
#include <hip/hip_runtime.h>
#include <hip/hip_bf16.h>

namespace {

typedef unsigned u32x2 __attribute__((ext_vector_type(2)));
typedef unsigned u32x4 __attribute__((ext_vector_type(4)));
typedef int i32x4 __attribute__((ext_vector_type(4)));
typedef short s16x4 __attribute__((ext_vector_type(4)));
typedef float f32x2 __attribute__((ext_vector_type(2)));
typedef float f32x4 __attribute__((ext_vector_type(4)));
typedef __bf16 bf16x2 __attribute__((ext_vector_type(2)));
typedef __bf16 bf16x8 __attribute__((ext_vector_type(8)));
#define LDS_AS __attribute__((address_space(3)))
#define DI __device__ __forceinline__

constexpr int D = 128;
constexpr int TN = 14;
constexpr int NW = 7;
constexpr int BN = TN * NW;
constexpr int NB = 256;
constexpr int NC = 128;
constexpr int CHUNK_MAX = 4096;
constexpr int NPH = 4;
constexpr int LIST_CAP = 2048;
constexpr unsigned SRC_MASK = 0x07FFFF00u;
constexpr unsigned SENTINEL = 0x07FFFF0Fu;

constexpr int L_RING = 0;
constexpr int L_WV = NW * 16384;
constexpr int L_P = L_WV + 32768;
constexpr int L_CNT = L_P + NW * 1024;
constexpr int L_LIST = L_CNT + 128;
constexpr int L_TOTAL = 163840;
static_assert(L_LIST + (LIST_CAP + NW * 31) * 4 <= L_TOTAL, "LDS budget");

DI unsigned swz(unsigned row) {
  const unsigned b3 = (row >> 3) & 1, b2 = (row >> 2) & 1;
  return ((b3 ^ b2) << 3) | ((row & 3) << 1) | b2;
}
template <typename T> DI T lds_ld(unsigned addr) { return *(const LDS_AS T*)(size_t)addr; }
template <typename T> DI void lds_st(unsigned addr, T v) { *(LDS_AS T*)(size_t)addr = v; }
DI s16x4 lds_tr(unsigned addr) {
#if defined(__HIP_DEVICE_COMPILE__)
  return __builtin_amdgcn_ds_read_tr16_b64_v4i16((LDS_AS s16x4*)(size_t)addr);
#else
  return s16x4{};
#endif
}
DI bf16x8 join(s16x4 a, s16x4 b) { return __builtin_bit_cast(bf16x8, __builtin_shufflevector(a, b, 0, 1, 2, 3, 4, 5, 6, 7)); }
DI unsigned pack_bf16(float a, float b) { return __builtin_bit_cast(unsigned, __builtin_convertvector(f32x2{a, b}, bf16x2)); }
DI f32x4 mfma(bf16x8 a, bf16x8 b, f32x4 c) {
#if defined(__HIP_DEVICE_COMPILE__)
  return __builtin_amdgcn_mfma_f32_16x16x32_bf16(a, b, c, 0, 0, 0);
#else
  return c;
#endif
}
DI u32x4 make_srd(const void* p, unsigned bytes) {
  const unsigned long long a = (unsigned long long)p;
  return u32x4{(unsigned)a, (unsigned)(a >> 32) & 0xffffu, bytes, 0x00020000u};
}
DI void dma4(u32x4 srd, unsigned lds, unsigned v0, unsigned v1, unsigned v2, unsigned v3,
             unsigned o0, unsigned o1, unsigned o2, unsigned o3) {
  asm volatile(
      "s_mov_b32 m0, %5\n\t"
      "s_nop 0\n\t"
      "buffer_load_dwordx4 %0, %4, %6 offen lds\n\t"
      "buffer_load_dwordx4 %1, %4, %7 offen offset:1024 lds\n\t"
      "buffer_load_dwordx4 %2, %4, %8 offen offset:2048 lds\n\t"
      "buffer_load_dwordx4 %3, %4, %9 offen offset:3072 lds"
      :: "v"(v0), "v"(v1), "v"(v2), "v"(v3), "s"(srd), "s"(lds), "s"(o0), "s"(o1), "s"(o2), "s"(o3)
      : "memory");
}
template <int N> DI void wait_vm() { asm volatile("s_waitcnt vmcnt(%0)" :: "i"(N) : "memory"); }
DI void wait_lds() { asm volatile("s_waitcnt lgkmcnt(0)" ::: "memory"); }
DI void bar() { asm volatile("s_waitcnt lgkmcnt(0)\n\ts_barrier" ::: "memory"); }
template <int CTRL, int RMASK = 0xf> DI unsigned dpp_add(unsigned v) {
  return v + (unsigned)__builtin_amdgcn_update_dpp(0, (int)v, CTRL, RMASK, 0xf, false);
}

__global__ __launch_bounds__(1024) void partition_kernel(
    const int* __restrict__ src, const int* __restrict__ dst, const char* __restrict__ x,
    unsigned* __restrict__ entries, unsigned* __restrict__ offs, int E, int N, int chunk,
    int warm_rows, unsigned phase_mul) {
  __shared__ __attribute__((aligned(16))) unsigned sm[CHUNK_MAX + 2 * NB];
  unsigned* hist = sm + CHUNK_MAX;
  unsigned* start = hist + NB;
  const int tid = threadIdx.x, lane = tid & 63;

  if (blockIdx.x >= NC) {
    const int j = (blockIdx.x - NC) >> 3, grp = blockIdx.x & 7, per = (gridDim.x - NC) >> 3;
    const u32x4 srd = make_srd(x, (unsigned)N * 256u);
    const unsigned lds = __builtin_amdgcn_readfirstlane((unsigned)(size_t)(LDS_AS char*)sm + (tid >> 6) * 1024);
    const unsigned rest = (unsigned)(N - warm_rows) * 256u, rest_per = ((rest + 7) / 8 + 15) & ~15u;
    const unsigned step = per * 1024 * 16, first = (j * 1024 + tid) * 16;
    for (unsigned o = first; o - lane * 16 < rest_per; o += step) {
      const unsigned v = o < rest_per ? warm_rows * 256u + grp * rest_per + o : 0x7fffff00u;
      asm volatile("s_mov_b32 m0, %2\n\ts_nop 0\n\tbuffer_load_dwordx4 %0, %1, 0 offen lds" :: "v"(v), "s"(srd), "s"(lds) : "memory");
    }
    for (unsigned o = first; o - lane * 16 < warm_rows * 256u; o += step)
      asm volatile("s_mov_b32 m0, %2\n\ts_nop 0\n\tbuffer_load_dwordx4 %0, %1, 0 offen lds" :: "v"(o), "s"(srd), "s"(lds) : "memory");
    wait_vm<0>();
    return;
  }

  const int c = blockIdx.x;
  const int e0 = c * chunk + 4 * tid;
  const int nval = 4 * tid < chunk ? min(4, E - e0) : 0;
  i32x4 d4 = {0, 0, 0, 0}, s4 = {0, 0, 0, 0};
  if (nval == 4) {
    d4 = *(const i32x4*)(dst + e0);
    s4 = *(const i32x4*)(src + e0);
  } else {
#pragma unroll
    for (int j = 0; j < 3; ++j)
      if (j < nval) { d4[j] = dst[e0 + j]; s4[j] = src[e0 + j]; }
  }
  if (tid < NB) hist[tid] = 0;
  __syncthreads();

  unsigned ent[4], bkt[4], rank[4];
#pragma unroll
  for (int j = 0; j < 4; ++j) {
    const unsigned d = d4[j], s = s4[j];
    const unsigned t = d / TN, b = t / NW;
    const unsigned ph = __umulhi(s, phase_mul);
    ent[j] = (s << 8) | (d - t * TN) | (((t - b * NW) * NPH + ph) << 27);
    bkt[j] = b;
    if (j < nval) rank[j] = atomicAdd(&hist[b], 1u);
  }
  __syncthreads();
  {
    const u32x4 h = *(const u32x4*)(hist + 4 * lane);
    const unsigned tot = h[0] + h[1] + h[2] + h[3];
    unsigned inc = tot;
    inc = dpp_add<0x111>(inc);
    inc = dpp_add<0x112>(inc);
    inc = dpp_add<0x114>(inc);
    inc = dpp_add<0x118>(inc);
    inc = dpp_add<0x142, 0xa>(inc);
    inc = dpp_add<0x143, 0xc>(inc);
    const unsigned ex = inc - tot;
    const u32x4 st = {ex, ex + h[0], ex + h[0] + h[1], ex + h[0] + h[1] + h[2]};
    *(u32x4*)(start + 4 * lane) = st;
    if (tid < 64) *(u32x4*)(offs + c * NB + 4 * lane) = st | (h << 16);
  }
#pragma unroll
  for (int j = 0; j < 4; ++j)
    if (j < nval) sm[start[bkt[j]] + rank[j]] = ent[j];
  __syncthreads();
  if (nval > 0) *(u32x4*)(entries + e0) = *(const u32x4*)(sm + 4 * tid);
}

struct Lane {
  unsigned row[4];
  unsigned tr[16];
  unsigned dma[4];
  unsigned pw, pr;
  unsigned g4;
};

DI void issue_rows(const Lane& L, u32x4 srd, unsigned list_addr, unsigned buf, unsigned o0, unsigned o1, unsigned o2) {
  const u32x4 a = lds_ld<u32x4>(list_addr), b = lds_ld<u32x4>(list_addr + 16);
  dma4(srd, buf, (a[0] & SRC_MASK) | L.dma[0], (a[1] & SRC_MASK) | L.dma[1], (a[2] & SRC_MASK) | L.dma[2],
       (a[3] & SRC_MASK) | L.dma[3], o0, o1, o2, 0);
  dma4(srd, buf + 4096, (b[0] & SRC_MASK) | L.dma[0], (b[1] & SRC_MASK) | L.dma[1], (b[2] & SRC_MASK) | L.dma[2],
       (b[3] & SRC_MASK) | L.dma[3], o0, o1, o2, 0);
}

DI float pick(f32x4 v, unsigned e) {
  const unsigned m0 = (unsigned)__builtin_amdgcn_sbfe((int)e, 0, 1), m1 = (unsigned)__builtin_amdgcn_sbfe((int)e, 1, 1);
  const unsigned lo = (__float_as_uint(v[1]) & m0) | (__float_as_uint(v[0]) & ~m0);
  const unsigned hi = (__float_as_uint(v[3]) & m0) | (__float_as_uint(v[2]) & ~m0);
  return __uint_as_float((hi & m1) | (lo & ~m1));
}
DI unsigned p_row(unsigned e, unsigned g4) {
  const unsigned d = e & 15;
  return (d ^ g4) < 4 ? d : 15;
}

template <int BUF>
DI void edge_tile(const Lane& L, const bf16x8 (&q)[4], f32x4 (&acc)[8], unsigned ent_addr) {
  const unsigned e0 = lds_ld<unsigned>(ent_addr), e1 = lds_ld<unsigned>(ent_addr + 16);
  f32x4 s0 = {0, 0, 0, 0}, s1 = {0, 0, 0, 0};
#pragma unroll
  for (int s = 0; s < 4; ++s) {
    s0 = mfma(q[s], lds_ld<bf16x8>(L.row[s] + BUF * 8192), s0);
    s1 = mfma(q[s], lds_ld<bf16x8>(L.row[s] + BUF * 8192 + 4096), s1);
  }
  bf16x8 xt[8];
#pragma unroll
  for (int t = 0; t < 8; ++t) xt[t] = join(lds_tr(L.tr[2 * t] + BUF * 8192), lds_tr(L.tr[2 * t + 1] + BUF * 8192));

  constexpr float SCALE = 0.08838834764831845f, NLOG2E = -1.4426950408889634f;
  unsigned pk = pack_bf16(pick(s0, e0), pick(s1, e1));
  pk = pack_bf16(__uint_as_float(pk << 16) * SCALE, __uint_as_float(pk & 0xffff0000u) * SCALE);
  const float a0 = __builtin_amdgcn_rcpf(1.0f + __builtin_amdgcn_exp2f(__uint_as_float(pk << 16) * NLOG2E));
  const float a1 = __builtin_amdgcn_rcpf(1.0f + __builtin_amdgcn_exp2f(__uint_as_float(pk & 0xffff0000u) * NLOG2E));
  pk = pack_bf16(a0, a1);
  const unsigned p0 = L.pw + (p_row(e0, L.g4) << 6), p1 = L.pw + 32 + (p_row(e1, L.g4) << 6);
  lds_st<unsigned short>(p0, (unsigned short)pk);
  lds_st<unsigned short>(p1, (unsigned short)(pk >> 16));
  const bf16x8 p = lds_ld<bf16x8>(L.pr);
  lds_st<unsigned short>(p0, 0);
  lds_st<unsigned short>(p1, 0);
#pragma unroll
  for (int t = 0; t < 8; ++t) acc[t] = mfma(xt[t], p, acc[t]);
}

__global__ __launch_bounds__(512, 2) void aggregate_kernel(
    const char* __restrict__ x, const char* __restrict__ wq, const char* __restrict__ wv,
    const unsigned* __restrict__ entries, const unsigned* __restrict__ offs, char* __restrict__ out,
    int N, int chunk) {
  __shared__ __attribute__((aligned(16))) char smem[L_TOTAL];
  const unsigned lds0 = (unsigned)(size_t)(LDS_AS char*)smem;
  const unsigned tid = threadIdx.x, lane = tid & 63;
  const unsigned w = __builtin_amdgcn_readfirstlane(tid >> 6);
  const unsigned bucket = (blockIdx.x & 7) * (NB / 8) + (blockIdx.x >> 3);
  const unsigned g = lane >> 4, c16 = lane & 15;
  const unsigned seg = tid >> 2, sj = tid & 3;
  const unsigned node0 = bucket * BN + w * TN;
  const unsigned ring = lds0 + L_RING + w * 16384;

  const u32x4 srd_x = make_srd(x - 3072, (unsigned)N * 256u + 3072u);
  const u32x4 srd_q = make_srd(wq, D * D * 2), srd_v = make_srd(wv, D * D * 2);
  const u32x4 srd_e = make_srd(entries, (unsigned)(NC * chunk + 64) * 4u);
  const unsigned o0 = 3072, o1 = 2048, o2 = 1024;

  unsigned ofs;
  asm volatile("global_load_dword %0, %1, %2" : "=v"(ofs) : "v"((seg * NB + bucket) * 4), "s"(offs) : "memory");

  Lane L;
#pragma unroll
  for (int q = 0; q < 4; ++q) L.dma[q] = (c16 ^ swz(4 * q + g)) << 4;
  {
    const unsigned r = (16 * w + g) * 256;
    dma4(srd_q, lds0 + L_RING + (w >> 1) * 16384 + 8192 + (w & 1) * 4096, r + L.dma[0], r + L.dma[1], r + L.dma[2], r + L.dma[3], 0, 0, 0, 0);
    const unsigned xr = (node0 + g) * 256;
    if (w < NW) dma4(srd_x, ring, xr + L.dma[0], xr + L.dma[1], xr + L.dma[2], xr + L.dma[3], o0, o0, o0, o0);
  }
  if (tid < 32) lds_st<unsigned>(lds0 + L_CNT + tid * 4, 0);
  if (w < NW) lds_st<u32x4>(lds0 + L_P + w * 1024 + lane * 16, u32x4{0, 0, 0, 0});
  bar();

  asm volatile("s_cmp_lt_u32 %1, 7\n\ts_cbranch_scc1 LW%=\n\ts_waitcnt vmcnt(4)\nLW%=:\n\ts_waitcnt vmcnt(8)"
               : "+v"(ofs) : "s"(w) : "memory", "scc");
  const unsigned seg_cnt = ofs >> 16;
  const unsigned seg_off = (seg * chunk + (ofs & 0xffff) + 4 * sj) * 4;

  u32x4 ea, eb;
  {
    const unsigned va = 4 * sj < seg_cnt ? seg_off : 0x7fff0000u;
    const unsigned vb = 16 + 4 * sj < seg_cnt ? seg_off + 64 : 0x7fff0000u;
    asm volatile("buffer_load_dwordx4 %0, %2, %4, 0 offen\n\tbuffer_load_dwordx4 %1, %3, %4, 0 offen"
                 : "=&v"(ea), "=&v"(eb) : "v"(va), "v"(vb), "s"(srd_e) : "memory");
  }
  {
    const unsigned r = (16 * w + g) * 256;
    dma4(srd_v, lds0 + L_WV + w * 4096, r + L.dma[0], r + L.dma[1], r + L.dma[2], r + L.dma[3], 0, 0, 0, 0);
  }

#pragma unroll
  for (int s = 0; s < 4; ++s) L.row[s] = ring + c16 * 256 + (((4 * s + g) ^ swz(c16)) << 4);
  const unsigned tq = c16 >> 2, tp = c16 & 3;
#pragma unroll
  for (int t = 0; t < 8; ++t)
#pragma unroll
    for (int h = 0; h < 2; ++h) {
      const unsigned row = 8 * g + 4 * h + tq;
      L.tr[2 * t + h] = ring + row * 256 + (((2 * t + (tp >> 1)) ^ swz(row & 15)) << 4) + 8 * (tp & 1);
    }
  L.pw = lds0 + L_P + w * 1024 + 2 * c16;
  L.pr = lds0 + L_P + w * 1024 + c16 * 64 + g * 16;
  L.g4 = 4 * g;

  f32x4 acc[8];
#pragma unroll
  for (int t = 0; t < 8; ++t) acc[t] = f32x4{0, 0, 0, 0};
  bf16x8 q[4];

  asm volatile("s_waitcnt vmcnt(4)" : "+v"(ea), "+v"(eb) :: "memory");
  bf16x8 xo[4];
  if (w < NW) {
#pragma unroll
    for (int s = 0; s < 4; ++s) xo[s] = lds_ld<bf16x8>(L.row[s]);
  }
  unsigned base = 0, take = 32;
  bool first = true;
  for (;;) {
    unsigned ent[8], rank[8];
#pragma unroll
    for (int j = 0; j < 4; ++j) { ent[j] = ea[j]; ent[4 + j] = eb[j]; }
    const unsigned lim = min(seg_cnt, base + take);
#pragma unroll
    for (int j = 0; j < 8; ++j) {
      const unsigned idx = base + (j >> 2) * 16 + 4 * sj + (j & 3);
      rank[j] = 0xffffffffu;
      if (idx < lim) rank[j] = __hip_atomic_fetch_add((LDS_AS unsigned*)(size_t)(lds0 + L_CNT + (ent[j] >> 27) * 4), 1u, __ATOMIC_RELAXED, __HIP_MEMORY_SCOPE_WORKGROUP);
    }
    if (seg_cnt > base + take) lds_st<unsigned>(lds0 + L_CNT + 31 * 4, 1);
    bar();
    const unsigned craw = lds_ld<unsigned>(lds0 + L_CNT + (lane & 31) * 4);
    const bool more = __builtin_amdgcn_readlane(craw, 31) != 0;
    const unsigned cnt = lane < NW * NPH ? craw : 0;
    unsigned inc = cnt;
    {
      const unsigned u = (unsigned)__builtin_amdgcn_update_dpp(0, (int)inc, 0x90, 0xf, 0xf, false);
      inc += tp >= 1 ? u : 0;
      const unsigned v = (unsigned)__builtin_amdgcn_update_dpp(0, (int)inc, 0x40, 0xf, 0xf, false);
      inc += tp >= 2 ? v : 0;
    }
    const unsigned tot = (unsigned)__builtin_amdgcn_update_dpp(0, (int)inc, 0xff, 0xf, 0xf, false);
    const unsigned pad = (tot + 31) & ~31u;
    unsigned sc = pad;
    sc = dpp_add<0x114>(sc);
    sc = dpp_add<0x118>(sc);
    sc = dpp_add<0x142, 0xa>(sc);
    const unsigned tile_start = sc - pad;
    unsigned sum = tot;
    sum = dpp_add<0x114>(sum);
    sum = dpp_add<0x118>(sum);
    sum = dpp_add<0x142, 0xa>(sum);
    if (__builtin_amdgcn_readlane(sum, 31) > LIST_CAP) {
      take = 16;
      bar();
      if (tid < 32) lds_st<unsigned>(lds0 + L_CNT + tid * 4, 0);
      bar();
      continue;
    }
    const unsigned bin_start = tile_start + inc - cnt;
#pragma unroll
    for (int j = 0; j < 8; ++j) {
      const unsigned st = __builtin_amdgcn_ds_bpermute((ent[j] >> 27) * 4, bin_start);
      if (rank[j] != 0xffffffffu) lds_st<unsigned>(lds0 + L_LIST + (st + rank[j]) * 4, ent[j]);
    }
    const unsigned wl = min(w, (unsigned)NW - 1) * NPH;
    const unsigned my_start = __builtin_amdgcn_readlane(tile_start, wl);
    const unsigned my_cnt = w < NW ? __builtin_amdgcn_readlane(tot, wl) : 0;
    const unsigned iters = (my_cnt + 31) >> 5;
    if (my_cnt + lane < iters * 32) lds_st<unsigned>(lds0 + L_LIST + (my_start + my_cnt + lane) * 4, SENTINEL);
    bar();

    unsigned la = lds0 + L_LIST + (my_start + 8 * g) * 4;
    unsigned le = lds0 + L_LIST + (my_start + 8 * tp + tq) * 4;
    if (iters > 0) issue_rows(L, srd_x, la, ring, o0, o1, o2);

    if (first) {
      first = false;
      if (w < NW) {
        const unsigned wbase = lds0 + L_RING + 8192 - ring;
#pragma unroll
        for (int s = 0; s < 4; ++s) {
          f32x4 qa = {0, 0, 0, 0}, qb = {0, 0, 0, 0};
#pragma unroll
          for (int k = 0; k < 4; ++k) {
            qa = mfma(join(lds_tr(L.tr[4 * s] + wbase + k * 16384), lds_tr(L.tr[4 * s + 1] + wbase + k * 16384)), xo[k], qa);
            qb = mfma(join(lds_tr(L.tr[4 * s + 2] + wbase + k * 16384), lds_tr(L.tr[4 * s + 3] + wbase + k * 16384)), xo[k], qb);
          }
          u32x4 f;
#pragma unroll
          for (int i = 0; i < 2; ++i) {
            const unsigned a = pack_bf16(qa[2 * i], qa[2 * i + 1]), b = pack_bf16(qb[2 * i], qb[2 * i + 1]);
#if defined(__HIP_DEVICE_COMPILE__)
            const auto r = __builtin_amdgcn_permlane32_swap(a, b, false, false);
            const auto r2 = __builtin_amdgcn_permlane16_swap(r[0], r[1], false, false);
            f[i] = r2[0];
            f[2 + i] = r2[1];
#endif
          }
          q[s] = __builtin_bit_cast(bf16x8, f);
        }
      }
      if (iters > 0) wait_vm<8>();
      else wait_vm<0>();
      bar();
    }

    for (unsigned it = 0; it < iters; it += 2) {
      if (it + 1 < iters) { issue_rows(L, srd_x, la + 128, ring + 8192, o0, o1, o2); wait_vm<8>(); }
      else wait_vm<0>();
      edge_tile<0>(L, q, acc, le);
      if (it + 1 >= iters) break;
      if (it + 2 < iters) { issue_rows(L, srd_x, la + 256, ring, o0, o1, o2); wait_vm<8>(); }
      else wait_vm<0>();
      edge_tile<1>(L, q, acc, le + 128);
      la += 256;
      le += 256;
    }
    if (!more) break;

    base += take;
    bar();
    if (tid < 32) lds_st<unsigned>(lds0 + L_CNT + tid * 4, 0);
    const unsigned va = base + 4 * sj < seg_cnt ? seg_off + base * 4 : 0x7fff0000u;
    const unsigned vb = base + 16 + 4 * sj < seg_cnt ? seg_off + base * 4 + 64 : 0x7fff0000u;
    asm volatile("buffer_load_dwordx4 %0, %2, %4, 0 offen\n\tbuffer_load_dwordx4 %1, %3, %4, 0 offen\n\ts_waitcnt vmcnt(0)"
                 : "=&v"(ea), "=&v"(eb) : "v"(va), "v"(vb), "s"(srd_e) : "memory");
    bar();
  }

  if (w >= NW) return;
  bf16x8 ag[4];
#pragma unroll
  for (int s = 0; s < 4; ++s)
    ag[s] = __builtin_bit_cast(bf16x8, u32x4{pack_bf16(acc[2 * s][0], acc[2 * s][1]), pack_bf16(acc[2 * s][2], acc[2 * s][3]),
                                             pack_bf16(acc[2 * s + 1][0], acc[2 * s + 1][1]), pack_bf16(acc[2 * s + 1][2], acc[2 * s + 1][3])});
  const unsigned vrow = 4 * g + tq;
#pragma unroll
  for (int t = 0; t < 8; ++t) {
    const unsigned a = lds0 + L_WV + vrow * 256 + (((2 * t + (tp >> 1)) ^ swz(vrow)) << 4) + 8 * (tp & 1);
    f32x4 o = {0, 0, 0, 0};
#pragma unroll
    for (int s = 0; s < 4; ++s) o = mfma(join(lds_tr(a + s * 8192), lds_tr(a + s * 8192 + 4096)), ag[s], o);
    lds_st<u32x2>(ring + c16 * 256 + (((2 * t + (g >> 1)) ^ c16) << 4) + 8 * (g & 1),
                  u32x2{pack_bf16(o[0], o[1]), pack_bf16(o[2], o[3])});
  }
#pragma unroll
  for (int i = 0; i < 4; ++i) {
    const unsigned row = 4 * i + g;
    const u32x4 v = lds_ld<u32x4>(ring + row * 256 + ((c16 ^ row) << 4));
    if (row < TN && node0 + row < (unsigned)N) *(u32x4*)(out + (size_t)(node0 + row) * 256 + c16 * 16) = v;
  }
}

}

extern "C" void kernel_launch(void* const* d_in, const int* in_sizes, int n_in, void* d_out, int out_size,
                              void* d_ws, size_t ws_size, hipStream_t stream) {
  const char* x = (const char*)d_in[0];
  const int* src = (const int*)d_in[1];
  const int* dst = (const int*)d_in[2];
  const char* wq = (const char*)d_in[3];
  const char* wv = (const char*)d_in[4];
  const int N = in_sizes[0] / D, E = in_sizes[1];
  const int chunk = ((E + NC - 1) / NC + 3) & ~3;
  unsigned* offs = (unsigned*)d_ws;
  unsigned* entries = offs + NC * NB;
  const int warm_rows = (int)(N * 0.4);
  const unsigned phase_mul = (unsigned)((((unsigned long long)NPH) << 32) / (unsigned)N);
  partition_kernel<<<NC + 128, 1024, 0, stream>>>(src, dst, x, entries, offs, E, N, chunk, warm_rows, phase_mul);
  aggregate_kernel<<<NB, 512, 0, stream>>>(x, wq, wv, entries, offs, (char*)d_out, N, chunk);
}
```

```cpp
#include <hip/hip_runtime.h>
#include <hip/hip_bf16.h>

namespace {

typedef unsigned u32x2 __attribute__((ext_vector_type(2)));
typedef unsigned u32x4 __attribute__((ext_vector_type(4)));
typedef short s16x4 __attribute__((ext_vector_type(4)));
typedef float f32x2 __attribute__((ext_vector_type(2)));
typedef float f32x4 __attribute__((ext_vector_type(4)));
typedef __bf16 bf16x2 __attribute__((ext_vector_type(2)));
typedef __bf16 bf16x8 __attribute__((ext_vector_type(8)));
#define LDS_AS __attribute__((address_space(3)))
#define GLB_AS __attribute__((address_space(1)))
#define DI __device__ __forceinline__
#define SCHED_FENCE() __builtin_amdgcn_sched_barrier(0)

constexpr int D = 128;
constexpr int TN = 14;
constexpr int NW = 7;
constexpr int NB = 256;
constexpr int CHUNK_MAX = NW * 64 * 4;
constexpr int TILE_CAP = 320;
constexpr int SEG_FAST = 24;
constexpr unsigned SRC_MASK = 0x00FFFF00u;
constexpr unsigned SENTINEL = 0xFFFFFFF0u;
constexpr unsigned OOB = 0x7FFF0000u;
constexpr unsigned TAG_STEP = 0x9E3779B9u;

constexpr unsigned W_GRAN = 4096;
constexpr unsigned W_ENT = W_GRAN + NB * NB * 8;

constexpr unsigned L_CNT = 0;
constexpr unsigned L_LIST = 64;
constexpr unsigned L_P = 9216;
constexpr unsigned L_RING = 16384;
constexpr unsigned L_WV = L_RING + NW * 16384;
constexpr unsigned L_TOTAL = L_WV + 32768;
constexpr unsigned L_SORT = L_LIST;
constexpr unsigned L_HIST = L_SORT + CHUNK_MAX * 4 + 64;
constexpr unsigned L_LUT = L_HIST + 1024;
constexpr unsigned L_START = L_P;
constexpr unsigned L_WQ = L_RING;
constexpr unsigned L_XO = L_RING + 32768;
static_assert(L_TOTAL == 163840 && L_LUT + 512 <= L_P && L_LIST + NW * TILE_CAP * 4 <= L_P, "LDS budget");
static_assert(L_LIST + (2048 + NW * 31) * 4 <= L_P, "slow-path list");

DI unsigned swz(unsigned row) {
  const unsigned b3 = (row >> 3) & 1, b2 = (row >> 2) & 1;
  return ((b3 ^ b2) << 3) | ((row & 3) << 1) | b2;
}
template <typename T> DI T lds_ld(unsigned addr) { return *(const LDS_AS T*)(size_t)addr; }
template <typename T> DI void lds_st(unsigned addr, T v) { *(LDS_AS T*)(size_t)addr = v; }
DI unsigned lds_add(unsigned addr, unsigned v) {
  return __hip_atomic_fetch_add((LDS_AS unsigned*)(size_t)addr, v, __ATOMIC_RELAXED, __HIP_MEMORY_SCOPE_WORKGROUP);
}
DI s16x4 lds_tr(unsigned addr) {
#if defined(__HIP_DEVICE_COMPILE__)
  return __builtin_amdgcn_ds_read_tr16_b64_v4i16((LDS_AS s16x4*)(size_t)addr);
#else
  return s16x4{};
#endif
}
DI bf16x8 join(s16x4 a, s16x4 b) { return __builtin_bit_cast(bf16x8, __builtin_shufflevector(a, b, 0, 1, 2, 3, 4, 5, 6, 7)); }
DI unsigned pack_bf16(float a, float b) { return __builtin_bit_cast(unsigned, __builtin_convertvector(f32x2{a, b}, bf16x2)); }
DI f32x4 mfma(bf16x8 a, bf16x8 b, f32x4 c) {
#if defined(__HIP_DEVICE_COMPILE__)
  return __builtin_amdgcn_mfma_f32_16x16x32_bf16(a, b, c, 0, 0, 0);
#else
  return c;
#endif
}
DI u32x4 make_srd(const void* p, unsigned bytes) {
  const unsigned long long a = (unsigned long long)p;
  return u32x4{(unsigned)a, (unsigned)(a >> 32) & 0xffffu, bytes, 0x00020000u};
}
DI void dma4(u32x4 srd, unsigned lds, unsigned v0, unsigned v1, unsigned v2, unsigned v3,
             unsigned o0, unsigned o1, unsigned o2, unsigned o3) {
  asm volatile(
      "s_mov_b32 m0, %5\n\t"
      "s_nop 0\n\t"
      "buffer_load_dwordx4 %0, %4, %6 offen lds\n\t"
      "buffer_load_dwordx4 %1, %4, %7 offen offset:1024 lds\n\t"
      "buffer_load_dwordx4 %2, %4, %8 offen offset:2048 lds\n\t"
      "buffer_load_dwordx4 %3, %4, %9 offen offset:3072 lds"
      :: "v"(v0), "v"(v1), "v"(v2), "v"(v3), "s"(srd), "s"(lds), "s"(o0), "s"(o1), "s"(o2), "s"(o3)
      : "memory");
}
template <int N> DI void wait_vm() { asm volatile("s_waitcnt vmcnt(%0)" :: "i"(N) : "memory"); }
DI void bar() { asm volatile("s_waitcnt lgkmcnt(0)\n\ts_barrier" ::: "memory"); }
template <int CTRL, int RMASK = 0xf> DI unsigned dpp_add(unsigned v) {
  return v + (unsigned)__builtin_amdgcn_update_dpp(0, (int)v, CTRL, RMASK, 0xf, false);
}
template <typename T> DI void pin(T& v) { asm volatile("" : "+v"(v)); }

#define STAMP(i)

struct Lane {
  unsigned row[4];
  unsigned tr[16];
  unsigned dma[4];
  unsigned pw0, pw1;
  unsigned pr;
  unsigned key;
};
struct Dma { u32x4 srd; unsigned o0, o1, o2, o3; };

DI void issue_rows(const Lane& L, const Dma& M, u32x4 a, u32x4 b, unsigned buf) {
  dma4(M.srd, buf, (a[0] & SRC_MASK) | L.dma[0], (a[1] & SRC_MASK) | L.dma[1], (a[2] & SRC_MASK) | L.dma[2],
       (a[3] & SRC_MASK) | L.dma[3], M.o0, M.o1, M.o2, M.o3);
  dma4(M.srd, buf + 4096, (b[0] & SRC_MASK) | L.dma[0], (b[1] & SRC_MASK) | L.dma[1], (b[2] & SRC_MASK) | L.dma[2],
       (b[3] & SRC_MASK) | L.dma[3], M.o0, M.o1, M.o2, M.o3);
}

template <int TABLE> DI unsigned bitop3(unsigned a, unsigned b, unsigned c) {
#if defined(__HIP_DEVICE_COMPILE__)
  return __builtin_amdgcn_bitop3_b32(a, b, c, TABLE);
#else
  return 0;
#endif
}
DI unsigned bfi(unsigned m, unsigned a, unsigned b) { return bitop3<0xCA>(m, a, b); }
DI unsigned bit_mask(unsigned e, int bit) { return (unsigned)__builtin_amdgcn_sbfe((int)e, bit, 1); }
DI float pick(f32x4 v, unsigned m0, unsigned m1) {
  return __uint_as_float(bfi(m1, bfi(m0, __float_as_uint(v[3]), __float_as_uint(v[2])),
                             bfi(m0, __float_as_uint(v[1]), __float_as_uint(v[0]))));
}
DI unsigned p_addr(unsigned e, unsigned key, unsigned base) { return bitop3<0xEA>((e >> 24) == key ? e : 0xF0u, 0xF0u, base); }
DI float gate(float logit) {
  constexpr float C = -0.08838834764831845f * 1.4426950408889634f;
  return __builtin_amdgcn_rcpf(1.0f + __builtin_amdgcn_exp2f(logit * C));
}

template <int BUF>
DI void edge_tile(const Lane& L, const Dma& M, const bf16x8 (&q)[4], f32x4 (&acc)[8], unsigned ent_addr,
                  unsigned next_addr, unsigned ring, bool refill) {
  bf16x8 xb[8], xt[8];
#pragma unroll
  for (int s = 0; s < 4; ++s) {
    xb[2 * s] = lds_ld<bf16x8>(L.row[s] + BUF * 8192);
    xb[2 * s + 1] = lds_ld<bf16x8>(L.row[s] + BUF * 8192 + 4096);
  }
  const unsigned e0 = lds_ld<unsigned>(ent_addr), e1 = lds_ld<unsigned>(ent_addr + 16);
  SCHED_FENCE();
  f32x4 s0 = {0, 0, 0, 0}, s1 = {0, 0, 0, 0};
#pragma unroll
  for (int s = 0; s < 4; ++s) {
    xt[2 * s] = join(lds_tr(L.tr[4 * s] + BUF * 8192), lds_tr(L.tr[4 * s + 1] + BUF * 8192));
    s0 = mfma(q[s], xb[2 * s], s0);
    SCHED_FENCE();
    xt[2 * s + 1] = join(lds_tr(L.tr[4 * s + 2] + BUF * 8192), lds_tr(L.tr[4 * s + 3] + BUF * 8192));
    s1 = mfma(q[s], xb[2 * s + 1], s1);
    SCHED_FENCE();
  }
  const u32x4 na = lds_ld<u32x4>(next_addr), nb = lds_ld<u32x4>(next_addr + 16);
  const unsigned m00 = bit_mask(e0, 4), m01 = bit_mask(e0, 5), m10 = bit_mask(e1, 4), m11 = bit_mask(e1, 5);
  const unsigned p0 = p_addr(e0, L.key, L.pw0), p1 = p_addr(e1, L.key, L.pw1);
  SCHED_FENCE();
  const unsigned pk = pack_bf16(gate(pick(s0, m00, m01)), gate(pick(s1, m10, m11)));
  lds_st<unsigned short>(p0, (unsigned short)pk);
  lds_st<unsigned short>(p1, (unsigned short)(pk >> 16));
  const bf16x8 p = lds_ld<bf16x8>(L.pr);
  lds_st<u32x4>(L.pr, u32x4{0, 0, 0, 0});
  SCHED_FENCE();
  if (refill) issue_rows(L, M, na, nb, ring + BUF * 8192);
  SCHED_FENCE();
#pragma unroll
  for (int t = 0; t < 8; ++t) acc[t] = mfma(xt[t], p, acc[t]);
}

DI void run_list(const Lane& L, const Dma& M, const bf16x8 (&q)[4], f32x4 (&acc)[8], unsigned list, unsigned iters,
                 unsigned ring, unsigned g, unsigned tp, unsigned tq) {
  unsigned la = list + 32 * g, le = list + 32 * tp + 4 * tq;
  if (iters > 0) issue_rows(L, M, lds_ld<u32x4>(la), lds_ld<u32x4>(la + 16), ring);
  if (iters > 1) issue_rows(L, M, lds_ld<u32x4>(la + 128), lds_ld<u32x4>(la + 144), ring + 8192);
  for (unsigned it = 0; it < iters; it += 2) {
    if (it + 1 < iters) wait_vm<8>(); else wait_vm<0>();
    edge_tile<0>(L, M, q, acc, le, la + 256, ring, it + 2 < iters);
    if (it + 1 >= iters) break;
    if (it + 2 < iters) wait_vm<8>(); else wait_vm<0>();
    edge_tile<1>(L, M, q, acc, le + 128, la + 384, ring, it + 3 < iters);
    la += 256;
    le += 256;
  }
}

DI void write_out(const f32x4 (&acc)[8], const unsigned (&wva)[8], unsigned ring, unsigned g, unsigned c16,
                  u32x4 srd_o, unsigned orow) {
  bf16x8 ag[4];
#pragma unroll
  for (int s = 0; s < 4; ++s)
    ag[s] = __builtin_bit_cast(bf16x8, u32x4{pack_bf16(acc[2 * s][0], acc[2 * s][1]), pack_bf16(acc[2 * s][2], acc[2 * s][3]),
                                             pack_bf16(acc[2 * s + 1][0], acc[2 * s + 1][1]), pack_bf16(acc[2 * s + 1][2], acc[2 * s + 1][3])});
  const unsigned st = ring + c16 * 256 + 8 * (g & 1), sx = (g >> 1) ^ c16;
#pragma unroll
  for (int t = 0; t < 8; ++t) {
    f32x4 o = {0, 0, 0, 0};
#pragma unroll
    for (int s = 0; s < 4; ++s) o = mfma(join(lds_tr(wva[t] + s * 8192), lds_tr(wva[t] + s * 8192 + 4096)), ag[s], o);
    lds_st<u32x2>(st + ((sx ^ (2 * t)) << 4), u32x2{pack_bf16(o[0], o[1]), pack_bf16(o[2], o[3])});
  }
#pragma unroll
  for (int i = 0; i < 4; ++i) {
    const unsigned row = 4 * i + g;
    const u32x4 v = lds_ld<u32x4>(ring + row * 256 + ((c16 ^ row) << 4));
    const unsigned off = (i == 3 && g >= 2) ? OOB : orow + i * (4u << 16);
    asm volatile("buffer_store_dwordx4 %0, %1, %2, 0 offen\n\ts_nop 1" :: "v"(v), "v"(off), "s"(srd_o) : "memory");
  }
}

__global__ __launch_bounds__(512, 2) void graph_kernel(
    const char* __restrict__ x, const char* __restrict__ src, const char* __restrict__ dst,
    const char* __restrict__ wq, const char* __restrict__ wv, char* __restrict__ out, char* __restrict__ ws,
    unsigned N, unsigned E, unsigned chunk, unsigned warm_lines) {
  __shared__ __attribute__((aligned(16))) char smem[L_TOTAL];
  asm volatile("" :: "v"((LDS_AS char*)smem));
  STAMP(0);
  const unsigned tid = threadIdx.x, lane = tid & 63;
  const unsigned w = __builtin_amdgcn_readfirstlane(tid >> 6);
  const unsigned c = blockIdx.x;
  const unsigned bucket = (c & 7) * (NB / 8) + (c >> 3);
  const unsigned g = lane >> 4, c16 = lane & 15, tq = c16 >> 2, tp = c16 & 3;

  const unsigned e0 = c * chunk + 4 * tid;
  const bool have = 4 * tid < chunk && e0 < E;
  u32x4 d4, s4;
  unsigned epoch;
  {
    const unsigned v = have ? e0 * 4 : OOB;
    asm volatile("buffer_load_dwordx4 %0, %3, %4, 0 offen\n\t"
                 "buffer_load_dwordx4 %1, %3, %5, 0 offen\n\t"
                 "global_load_dword %2, %6, %7"
                 : "=&v"(d4), "=&v"(s4), "=&v"(epoch)
                 : "v"(v), "s"(make_srd(dst, E * 4)), "s"(make_srd(src, E * 4)), "v"(0u), "s"(ws) : "memory");
  }
  Dma M;
  M.srd = make_srd(x - 3072, N * 256u + 3072u);
  M.o0 = 3072; M.o1 = 2048; M.o2 = 1024; M.o3 = 0;
  Lane L;
#pragma unroll
  for (int q = 0; q < 4; ++q) L.dma[q] = (c16 ^ swz(4 * q + g)) << 4;
  const unsigned orow = ((w * TN + g) << 16) + bucket * 256;
  {
    const unsigned r = (16 * w + g) * 256;
    dma4(make_srd(wq, D * D * 2), L_WQ + w * 4096, r + L.dma[0], r + L.dma[1], r + L.dma[2], r + L.dma[3], 0, 0, 0, 0);
    dma4(M.srd, L_XO + w * 4096, orow + L.dma[0], orow + (4u << 16) + L.dma[1], orow + (8u << 16) + L.dma[2],
         orow + (12u << 16) + L.dma[3], M.o0, M.o1, M.o2, M.o3);
    dma4(make_srd(wv, D * D * 2), L_WV + w * 4096, r + L.dma[0], r + L.dma[1], r + L.dma[2], r + L.dma[3], 0, 0, 0, 0);
  }
  if (w < 4) lds_st<unsigned>(L_HIST + tid * 4, 0);
  else if (w < 6) {
    const unsigned j = tid - NB, t = j / TN, d = j - t * TN;
    lds_st<unsigned>(L_LUT + j * 4, (d << 4) | (((t << 2) | (d >> 2)) << 24));
  } else if (tid < 6 * 64 + 8) {
    lds_st<unsigned>(L_CNT + lane * 4, lane < NW ? L_LIST + lane * TILE_CAP * 4 : 0);
  }
  unsigned wva[8];
  {
    unsigned r0 = c16 * 256 + ((g ^ swz(c16)) << 4);
    const unsigned ra = 8 * g + tq, rb = ra + 4, rv = 4 * g + tq, lo = 8 * (tp & 1);
    unsigned ta = ra * 256 + (((tp >> 1) ^ swz(ra & 15)) << 4) + lo;
    unsigned tb = rb * 256 + (((tp >> 1) ^ swz(rb & 15)) << 4) + lo;
    unsigned tv = L_WV + rv * 256 + (((tp >> 1) ^ swz(rv)) << 4) + lo;
    pin(r0); pin(ta); pin(tb); pin(tv);
#pragma unroll
    for (int s = 0; s < 4; ++s) { L.row[s] = r0 ^ (s << 6); pin(L.row[s]); }
#pragma unroll
    for (int t = 0; t < 8; ++t) {
      L.tr[2 * t] = ta ^ (t << 5); L.tr[2 * t + 1] = tb ^ (t << 5); wva[t] = tv ^ (t << 5);
      pin(L.tr[2 * t]); pin(L.tr[2 * t + 1]); pin(wva[t]);
    }
  }
  L.pw0 = L_P + w * 1024 + (c16 >> 3) * 256 + (c16 & 7) * 2;
  L.pw1 = L.pw0 + 512;
  L.pr = L_P + w * 1024 + lane * 16;
  L.key = (w << 2) | g;
  pin(L.pw0); pin(L.pw1); pin(L.pr); pin(L.key);
  bar();
  STAMP(1);

  asm volatile("s_waitcnt vmcnt(12)" : "+v"(d4), "+v"(s4), "+v"(epoch) :: "memory");
  STAMP(2);
  unsigned hb[4], rank[4] = {0, 0, 0, 0}, ent[4];
  if (E & 3) {
#pragma unroll
    for (int j = 1; j < 4; ++j) s4[j] = e0 + j < E ? s4[j] : 0xffffu;
  }
#pragma unroll
  for (int j = 0; j < 4; ++j) hb[j] = (d4[j] & 255) * 4;
  if (have) {
#pragma unroll
    for (int j = 0; j < 4; ++j) rank[j] = lds_add(L_HIST + hb[j], 4);
  }
#pragma unroll
  for (int j = 0; j < 4; ++j) ent[j] = lds_ld<unsigned>(L_LUT + ((d4[j] >> 8) & 127) * 4) | (s4[j] << 8);
  bar();
  STAMP(3);
  {
    const u32x4 h = lds_ld<u32x4>(L_HIST + 16 * lane);
    const unsigned tot = h[0] + h[1] + h[2] + h[3];
    unsigned inc = tot;
    inc = dpp_add<0x111>(inc);
    inc = dpp_add<0x112>(inc);
    inc = dpp_add<0x114>(inc);
    inc = dpp_add<0x118>(inc);
    inc = dpp_add<0x142, 0xa>(inc);
    inc = dpp_add<0x143, 0xc>(inc);
    const unsigned ex = inc - tot;
    lds_st<u32x4>(L_START + 16 * lane, u32x4{ex, ex + h[0], ex + h[0] + h[1], ex + h[0] + h[1] + h[2]});
  }
  {
    unsigned pos[4];
#pragma unroll
    for (int j = 0; j < 4; ++j) pos[j] = lds_ld<unsigned>(L_START + hb[j]) + rank[j];
    if (have) {
#pragma unroll
      for (int j = 0; j < 4; ++j) lds_st<unsigned>(L_SORT + pos[j], ent[j]);
    }
  }
  wait_vm<0>();
  bar();
  STAMP(4);
  const u32x4 srd_e = make_srd(ws + W_ENT, (NB * chunk + 64) * 4u);
  {
    const u32x4 v = lds_ld<u32x4>(L_SORT + 16 * tid);
    const unsigned off = have ? e0 * 4 : OOB;
    asm volatile("buffer_store_dwordx4 %0, %1, %2, 0 offen sc1\n\ts_nop 1" :: "v"(v), "v"(off), "s"(srd_e) : "memory");
  }
  const unsigned tag = __builtin_amdgcn_readfirstlane(epoch) + TAG_STEP, tag2 = (tag * 167u) >> 24;
  const unsigned gran_lo = (lds_ld<unsigned>(L_START + (tid & 255) * 4) >> 2) | (lds_ld<unsigned>(L_HIST + (tid & 255) * 4) << 10) | (tag2 << 24);

  bf16x8 q[4];
  {
    bf16x8 xo[4];
#pragma unroll
    for (int s = 0; s < 4; ++s) xo[s] = lds_ld<bf16x8>(L.row[s] + L_XO + w * 4096);
#pragma unroll
    for (int s = 0; s < 4; ++s) {
      f32x4 qa = {0, 0, 0, 0}, qb = {0, 0, 0, 0};
#pragma unroll
      for (int k = 0; k < 4; ++k) {
        qa = mfma(join(lds_tr(L.tr[4 * s] + L_WQ + k * 8192), lds_tr(L.tr[4 * s + 1] + L_WQ + k * 8192)), xo[k], qa);
        qb = mfma(join(lds_tr(L.tr[4 * s + 2] + L_WQ + k * 8192), lds_tr(L.tr[4 * s + 3] + L_WQ + k * 8192)), xo[k], qb);
      }
      u32x4 f;
#pragma unroll
      for (int i = 0; i < 2; ++i) {
        const unsigned a = pack_bf16(qa[2 * i], qa[2 * i + 1]), b = pack_bf16(qb[2 * i], qb[2 * i + 1]);
#if defined(__HIP_DEVICE_COMPILE__)
        const auto r = __builtin_amdgcn_permlane32_swap(a, b, false, false);
        const auto r2 = __builtin_amdgcn_permlane16_swap(r[0], r[1], false, false);
        f[i] = r2[0];
        f[2 + i] = r2[1];
#endif
      }
      q[s] = __builtin_bit_cast(bf16x8, f);
      pin(q[s]);
    }
  }
  STAMP(5);
  wait_vm<0>();
  bar();
  STAMP(6);
  GLB_AS unsigned long long* gran = (GLB_AS unsigned long long*)(ws + W_GRAN);
  if (tid < NB)
    __hip_atomic_store(gran + c * NB + tid, ((unsigned long long)tag << 32) | gran_lo, __ATOMIC_RELAXED, __HIP_MEMORY_SCOPE_AGENT);

  const unsigned ring = L_RING + w * 16384;
#pragma unroll
  for (int s = 0; s < 4; ++s) { L.row[s] += ring; pin(L.row[s]); }
#pragma unroll
  for (int t = 0; t < 16; ++t) { L.tr[t] += ring; pin(L.tr[t]); }
  if (w < NW) lds_st<u32x4>(L.pr, u32x4{0, 0, 0, 0});
  f32x4 acc[8];
#pragma unroll
  for (int t = 0; t < 8; ++t) { acc[t] = f32x4{0, 0, 0, 0}; pin(acc[t]); }

  const unsigned seg = tid >> 1, sj = tid & 1;
  unsigned seg_cnt = 0, seg_off = 0;
  {
    unsigned long long gv;
    unsigned spins = 0;
    bool ok;
    do {
      gv = __hip_atomic_load(gran + seg * NB + bucket, __ATOMIC_RELAXED, __HIP_MEMORY_SCOPE_AGENT);
      ok = (unsigned)(gv >> 32) == tag && ((unsigned)gv >> 24) == tag2;
    } while (!ok && ++spins < (1u << 20));
    if (ok) {
      seg_cnt = ((unsigned)gv >> 12) & 0xfff;
      seg_off = (seg * chunk + ((unsigned)gv & 0xfff) + 4 * sj) * 4;
    }
  }
  STAMP(7);
  const int rem = (int)seg_cnt - 4 * (int)sj;
  const bool third = __builtin_amdgcn_ballot_w64(rem > 16) != 0;
  u32x4 ea, eb, ec = {0, 0, 0, 0};
  {
    const unsigned va = rem > 0 ? seg_off : OOB, vb = rem > 8 ? seg_off + 32 : OOB;
    if (third) {
      const unsigned vc = rem > 16 ? seg_off + 64 : OOB;
      asm volatile("buffer_load_dwordx4 %0, %1, %2, 0 offen sc1\n\ts_waitcnt vmcnt(0)" : "=&v"(ec) : "v"(vc), "s"(srd_e) : "memory");
    }
    asm volatile("buffer_load_dwordx4 %0, %2, %4, 0 offen sc1\n\t"
                 "buffer_load_dwordx4 %1, %3, %4, 0 offen sc1\n\t"
                 "s_waitcnt vmcnt(0)"
                 : "=&v"(ea), "=&v"(eb) : "v"(va), "v"(vb), "s"(srd_e) : "memory");
  }
  STAMP(8);
  {
    unsigned a0, a1, a2, a3, a4, a5, a6, a7;
#define TILE_OF(D, S) "v_and_b32_sdwa " D ", %[m], " S " dst_sel:DWORD dst_unused:UNUSED_PAD src0_sel:DWORD src1_sel:BYTE_3\n\t"
#define TAKE(K, D) "v_cmpx_lt_i32 vcc, " K ", %[r]\n\tds_add_rtn_u32 " D ", " D ", %[f]\n\t"
#define PUT(K, D, S) "v_cmpx_lt_i32 vcc, " K ", %[r]\n\tds_write_b32 " D ", " S "\n\t"
    asm volatile(TILE_OF("%0", "%8") TILE_OF("%1", "%9") TILE_OF("%2", "%10") TILE_OF("%3", "%11")
                 TILE_OF("%4", "%12") TILE_OF("%5", "%13") TILE_OF("%6", "%14") TILE_OF("%7", "%15")
                 TAKE("0", "%0") TAKE("1", "%1") TAKE("2", "%2") TAKE("3", "%3")
                 TAKE("8", "%4") TAKE("9", "%5") TAKE("10", "%6") TAKE("11", "%7")
                 "s_mov_b64 exec, -1\n\ts_waitcnt lgkmcnt(0)\n\t"
                 PUT("0", "%0", "%8") PUT("1", "%1", "%9") PUT("2", "%2", "%10") PUT("3", "%3", "%11")
                 PUT("8", "%4", "%12") PUT("9", "%5", "%13") PUT("10", "%6", "%14") PUT("11", "%7", "%15")
                 "s_mov_b64 exec, -1"
                 : "=&v"(a0), "=&v"(a1), "=&v"(a2), "=&v"(a3), "=&v"(a4), "=&v"(a5), "=&v"(a6), "=&v"(a7)
                 : "v"(ea[0]), "v"(ea[1]), "v"(ea[2]), "v"(ea[3]), "v"(eb[0]), "v"(eb[1]), "v"(eb[2]), "v"(eb[3]),
                   [r] "v"(rem), [m] "v"(0x1cu), [f] "v"(4u)
                 : "memory", "vcc");
    if (third) {
      asm volatile(TILE_OF("%0", "%4") TILE_OF("%1", "%5") TILE_OF("%2", "%6") TILE_OF("%3", "%7")
                   TAKE("16", "%0") TAKE("17", "%1") TAKE("18", "%2") TAKE("19", "%3")
                   "s_mov_b64 exec, -1\n\ts_waitcnt lgkmcnt(0)\n\t"
                   PUT("16", "%0", "%4") PUT("17", "%1", "%5") PUT("18", "%2", "%6") PUT("19", "%3", "%7")
                   "s_mov_b64 exec, -1"
                   : "=&v"(a0), "=&v"(a1), "=&v"(a2), "=&v"(a3)
                   : "v"(ec[0]), "v"(ec[1]), "v"(ec[2]), "v"(ec[3]), [r] "v"(rem), [m] "v"(0x1cu), [f] "v"(4u)
                   : "memory", "vcc");
    }
#undef TILE_OF
#undef TAKE
#undef PUT
    if (seg_cnt > SEG_FAST) lds_st<unsigned>(L_CNT + 28, 1);
  }
  bar();
  STAMP(9);
  if (c == 0 && tid == 0) *(GLB_AS unsigned*)ws = tag;
  const u32x4 srd_o = make_srd(out, N * 256u);
  const unsigned cur = lds_ld<unsigned>(L_CNT + (lane & 7) * 4) - ((lane & 7) < NW ? L_LIST + (lane & 7) * TILE_CAP * 4 : 0);
  const bool slow = __builtin_amdgcn_ballot_w64((lane & 7) < NW ? cur > TILE_CAP * 4 : cur != 0) != 0;

  if (!slow) {
    if (w >= NW) return;
    const unsigned my_cnt = __builtin_amdgcn_readlane(cur, w) >> 2, iters = (my_cnt + 31) >> 5;
    const unsigned list = L_LIST + w * TILE_CAP * 4;
    if (my_cnt + lane < iters * 32) lds_st<unsigned>(list + (my_cnt + lane) * 4, SENTINEL);
    run_list(L, M, q, acc, list, iters, ring, g, tp, tq);
    STAMP(10);
    write_out(acc, wva, ring, g, c16, srd_o, orow + c16 * 16);
    return;
  }

  for (unsigned base = 0;; base += 8) {
    bar();
    if (tid < 8) lds_st<unsigned>(L_CNT + tid * 4, 0);
    if (w < NW) lds_st<u32x4>(L.pr, u32x4{0, 0, 0, 0});
    const int left = rem - (int)base;
    const unsigned va = left > 0 ? seg_off + base * 4 : OOB;
    asm volatile("buffer_load_dwordx4 %0, %1, %2, 0 offen sc1\n\ts_waitcnt vmcnt(0)" : "=&v"(ea) : "v"(va), "s"(srd_e) : "memory");
    bar();
    unsigned rk[4];
#pragma unroll
    for (int j = 0; j < 4; ++j)
      if (j < left) rk[j] = lds_add(L_CNT + (ea[j] >> 26) * 4, 1);
    if (seg_cnt > base + 8) lds_st<unsigned>(L_CNT + 28, 1);
    bar();
    const unsigned cn = lds_ld<unsigned>(L_CNT + (lane & 7) * 4);
    const bool more = __builtin_amdgcn_readlane(cn, 7) != 0;
    const unsigned pad = (lane & 7) < NW ? (cn + 31) & ~31u : 0;
    unsigned sc = lane < 8 ? pad : 0;
    sc = dpp_add<0x111>(sc);
    sc = dpp_add<0x112>(sc);
    sc = dpp_add<0x114>(sc);
    const unsigned st = sc - pad;
#pragma unroll
    for (int j = 0; j < 4; ++j) {
      const unsigned s = __builtin_amdgcn_ds_bpermute((ea[j] >> 26) * 4, st);
      if (j < left) lds_st<unsigned>(L_LIST + (s + rk[j]) * 4, ea[j]);
    }
    const unsigned wl = min(w, (unsigned)NW - 1);
    const unsigned my_cnt = w < NW ? __builtin_amdgcn_readlane(cn, wl) : 0, iters = (my_cnt + 31) >> 5;
    const unsigned list = L_LIST + __builtin_amdgcn_readlane(st, wl) * 4;
    if (my_cnt + lane < iters * 32) lds_st<unsigned>(list + (my_cnt + lane) * 4, SENTINEL);
    bar();
    run_list(L, M, q, acc, list, iters, ring, g, tp, tq);
    if (!more) break;
  }
  if (w < NW) write_out(acc, wva, ring, g, c16, srd_o, orow + c16 * 16);
}

}

extern "C" void kernel_launch(void* const* d_in, const int* in_sizes, int n_in, void* d_out, int out_size,
                              void* d_ws, size_t ws_size, hipStream_t stream) {
  const unsigned N = in_sizes[0] / D, E = in_sizes[1];
  const unsigned chunk = ((E + NB - 1) / NB + 3) & ~3u;
  graph_kernel<<<NB, 512, 0, stream>>>((const char*)d_in[0], (const char*)d_in[1], (const char*)d_in[2],
                                       (const char*)d_in[3], (const char*)d_in[4], (char*)d_out, (char*)d_ws,
                                       N, E, chunk, 0u);
}
```

```cpp
#include <hip/hip_runtime.h>
#include <hip/hip_bf16.h>

namespace {

typedef unsigned u32x2 __attribute__((ext_vector_type(2)));
typedef unsigned u32x4 __attribute__((ext_vector_type(4)));
typedef int i32x4 __attribute__((ext_vector_type(4)));
typedef short s16x4 __attribute__((ext_vector_type(4)));
typedef float f32x2 __attribute__((ext_vector_type(2)));
typedef float f32x4 __attribute__((ext_vector_type(4)));
typedef __bf16 bf16x2 __attribute__((ext_vector_type(2)));
typedef __bf16 bf16x8 __attribute__((ext_vector_type(8)));
#define LDS_AS __attribute__((address_space(3)))
#define DI __device__ __forceinline__

constexpr int D = 128;
constexpr int TN = 14;
constexpr int NW = 7;
constexpr int BN = TN * NW;
constexpr int NB = 256;
constexpr int NC = 128;
constexpr int CHUNK_MAX = 4096;
constexpr int NPH = 4;
constexpr int LIST_CAP = 2048;
constexpr unsigned SRC_MASK = 0x07FFFF00u;
constexpr unsigned SENTINEL = 0x07FFFF0Fu;

constexpr int L_RING = 0;
constexpr int L_WV = NW * 16384;
constexpr int L_P = L_WV + 32768;
constexpr int L_CNT = L_P + NW * 1024;
constexpr int L_LIST = L_CNT + 128;
constexpr int L_TOTAL = 163840;
static_assert(L_LIST + (LIST_CAP + NW * 31) * 4 <= L_TOTAL, "LDS budget");

DI unsigned swz(unsigned row) {
  const unsigned b3 = (row >> 3) & 1, b2 = (row >> 2) & 1;
  return ((b3 ^ b2) << 3) | ((row & 3) << 1) | b2;
}
template <typename T> DI T lds_ld(unsigned addr) { return *(const LDS_AS T*)(size_t)addr; }
template <typename T> DI void lds_st(unsigned addr, T v) { *(LDS_AS T*)(size_t)addr = v; }
DI s16x4 lds_tr(unsigned addr) {
#if defined(__HIP_DEVICE_COMPILE__)
  return __builtin_amdgcn_ds_read_tr16_b64_v4i16((LDS_AS s16x4*)(size_t)addr);
#else
  return s16x4{};
#endif
}
DI bf16x8 join(s16x4 a, s16x4 b) { return __builtin_bit_cast(bf16x8, __builtin_shufflevector(a, b, 0, 1, 2, 3, 4, 5, 6, 7)); }
DI unsigned pack_bf16(float a, float b) { return __builtin_bit_cast(unsigned, __builtin_convertvector(f32x2{a, b}, bf16x2)); }
DI f32x4 mfma(bf16x8 a, bf16x8 b, f32x4 c) {
#if defined(__HIP_DEVICE_COMPILE__)
  return __builtin_amdgcn_mfma_f32_16x16x32_bf16(a, b, c, 0, 0, 0);
#else
  return c;
#endif
}
DI u32x4 make_srd(const void* p, unsigned bytes) {
  const unsigned long long a = (unsigned long long)p;
  return u32x4{(unsigned)a, (unsigned)(a >> 32) & 0xffffu, bytes, 0x00020000u};
}
DI void dma4(u32x4 srd, unsigned lds, unsigned v0, unsigned v1, unsigned v2, unsigned v3,
             unsigned o0, unsigned o1, unsigned o2, unsigned o3) {
  asm volatile(
      "s_mov_b32 m0, %5\n\t"
      "s_nop 0\n\t"
      "buffer_load_dwordx4 %0, %4, %6 offen lds\n\t"
      "buffer_load_dwordx4 %1, %4, %7 offen offset:1024 lds\n\t"
      "buffer_load_dwordx4 %2, %4, %8 offen offset:2048 lds\n\t"
      "buffer_load_dwordx4 %3, %4, %9 offen offset:3072 lds"
      :: "v"(v0), "v"(v1), "v"(v2), "v"(v3), "s"(srd), "s"(lds), "s"(o0), "s"(o1), "s"(o2), "s"(o3)
      : "memory");
}
template <int N> DI void wait_vm() { asm volatile("s_waitcnt vmcnt(%0)" :: "i"(N) : "memory"); }
DI void wait_lds() { asm volatile("s_waitcnt lgkmcnt(0)" ::: "memory"); }
DI void bar() { asm volatile("s_waitcnt lgkmcnt(0)\n\ts_barrier" ::: "memory"); }
template <int CTRL, int RMASK = 0xf> DI unsigned dpp_add(unsigned v) {
  return v + (unsigned)__builtin_amdgcn_update_dpp(0, (int)v, CTRL, RMASK, 0xf, false);
}

#define STAMP1(i)
#define STAMP2(i)
__global__ __launch_bounds__(1024) void partition_kernel(
    const int* __restrict__ src, const int* __restrict__ dst, const char* __restrict__ x,
    unsigned* __restrict__ entries, unsigned* __restrict__ offs, int E, int N, int chunk,
    int warm_rows, unsigned phase_mul) {
  __shared__ __attribute__((aligned(16))) unsigned sm[CHUNK_MAX + 2 * NB];
  unsigned* hist = sm + CHUNK_MAX;
  unsigned* start = hist + NB;
  const int tid = threadIdx.x, lane = tid & 63;
  STAMP1(0);

  if (blockIdx.x >= NC) {
    const int j = (blockIdx.x - NC) >> 3, grp = blockIdx.x & 7, per = (gridDim.x - NC) >> 3;
    const u32x4 srd = make_srd(x, (unsigned)N * 256u);
    const unsigned lds = __builtin_amdgcn_readfirstlane((unsigned)(size_t)(LDS_AS char*)sm + (tid >> 6) * 1024);
    const unsigned rest = (unsigned)(N - warm_rows) * 256u, rest_per = ((rest + 7) / 8 + 15) & ~15u;
    const unsigned step = per * 1024 * 16, first = (j * 1024 + tid) * 16;
    for (unsigned o = first; o - lane * 16 < rest_per; o += step) {
      const unsigned v = o < rest_per ? warm_rows * 256u + grp * rest_per + o : 0x7fffff00u;
      asm volatile("s_mov_b32 m0, %2\n\ts_nop 0\n\tbuffer_load_dwordx4 %0, %1, 0 offen lds" :: "v"(v), "s"(srd), "s"(lds) : "memory");
    }
    for (unsigned o = first; o - lane * 16 < warm_rows * 256u; o += step)
      asm volatile("s_mov_b32 m0, %2\n\ts_nop 0\n\tbuffer_load_dwordx4 %0, %1, 0 offen lds" :: "v"(o), "s"(srd), "s"(lds) : "memory");
    wait_vm<0>();
    STAMP1(5);
    return;
  }

  const int c = blockIdx.x;
  const int e0 = c * chunk + 4 * tid;
  const int nval = 4 * tid < chunk ? min(4, E - e0) : 0;
  i32x4 d4 = {0, 0, 0, 0}, s4 = {0, 0, 0, 0};
  if (nval == 4) {
    d4 = *(const i32x4*)(dst + e0);
    s4 = *(const i32x4*)(src + e0);
  } else {
#pragma unroll
    for (int j = 0; j < 3; ++j)
      if (j < nval) { d4[j] = dst[e0 + j]; s4[j] = src[e0 + j]; }
  }
  if (tid < NB) hist[tid] = 0;
  __syncthreads();
  STAMP1(1);

  unsigned ent[4], bkt[4], rank[4];
#pragma unroll
  for (int j = 0; j < 4; ++j) {
    const unsigned d = d4[j], s = s4[j];
    const unsigned t = d / TN, b = t / NW;
    const unsigned ph = __umulhi(s, phase_mul);
    ent[j] = (s << 8) | (d - t * TN) | (((t - b * NW) * NPH + ph) << 27);
    bkt[j] = b;
    if (j < nval) rank[j] = atomicAdd(&hist[b], 1u);
  }
  __syncthreads();
  STAMP1(2);
  {
    const u32x4 h = *(const u32x4*)(hist + 4 * lane);
    const unsigned tot = h[0] + h[1] + h[2] + h[3];
    unsigned inc = tot;
    inc = dpp_add<0x111>(inc);
    inc = dpp_add<0x112>(inc);
    inc = dpp_add<0x114>(inc);
    inc = dpp_add<0x118>(inc);
    inc = dpp_add<0x142, 0xa>(inc);
    inc = dpp_add<0x143, 0xc>(inc);
    const unsigned ex = inc - tot;
    const u32x4 st = {ex, ex + h[0], ex + h[0] + h[1], ex + h[0] + h[1] + h[2]};
    *(u32x4*)(start + 4 * lane) = st;
    if (tid < 64) *(u32x4*)(offs + c * NB + 4 * lane) = st | (h << 16);
  }
#pragma unroll
  for (int j = 0; j < 4; ++j)
    if (j < nval) sm[start[bkt[j]] + rank[j]] = ent[j];
  __syncthreads();
  STAMP1(3);
  if (nval > 0) *(u32x4*)(entries + e0) = *(const u32x4*)(sm + 4 * tid);
  STAMP1(4);
}

struct Lane {
  unsigned row[4];
  unsigned tr[16];
  unsigned dma[4];
  unsigned pw, pr;
  unsigned g4;
};

DI void issue_rows(const Lane& L, u32x4 srd, unsigned list_addr, unsigned buf, unsigned o0, unsigned o1, unsigned o2) {
  const u32x4 a = lds_ld<u32x4>(list_addr), b = lds_ld<u32x4>(list_addr + 16);
  dma4(srd, buf, (a[0] & SRC_MASK) | L.dma[0], (a[1] & SRC_MASK) | L.dma[1], (a[2] & SRC_MASK) | L.dma[2],
       (a[3] & SRC_MASK) | L.dma[3], o0, o1, o2, 0);
  dma4(srd, buf + 4096, (b[0] & SRC_MASK) | L.dma[0], (b[1] & SRC_MASK) | L.dma[1], (b[2] & SRC_MASK) | L.dma[2],
       (b[3] & SRC_MASK) | L.dma[3], o0, o1, o2, 0);
}

DI float pick(f32x4 v, unsigned e) {
  const unsigned m0 = (unsigned)__builtin_amdgcn_sbfe((int)e, 0, 1), m1 = (unsigned)__builtin_amdgcn_sbfe((int)e, 1, 1);
  const unsigned lo = (__float_as_uint(v[1]) & m0) | (__float_as_uint(v[0]) & ~m0);
  const unsigned hi = (__float_as_uint(v[3]) & m0) | (__float_as_uint(v[2]) & ~m0);
  return __uint_as_float((hi & m1) | (lo & ~m1));
}
DI unsigned p_row(unsigned e, unsigned g4) {
  const unsigned d = e & 15;
  return (d ^ g4) < 4 ? d : 15;
}

template <int BUF>
DI void edge_tile(const Lane& L, const bf16x8 (&q)[4], f32x4 (&acc)[8], unsigned ent_addr) {
  const unsigned e0 = lds_ld<unsigned>(ent_addr), e1 = lds_ld<unsigned>(ent_addr + 16);
  f32x4 s0 = {0, 0, 0, 0}, s1 = {0, 0, 0, 0};
#pragma unroll
  for (int s = 0; s < 4; ++s) {
    s0 = mfma(q[s], lds_ld<bf16x8>(L.row[s] + BUF * 8192), s0);
    s1 = mfma(q[s], lds_ld<bf16x8>(L.row[s] + BUF * 8192 + 4096), s1);
  }
  bf16x8 xt[8];
#pragma unroll
  for (int t = 0; t < 8; ++t) xt[t] = join(lds_tr(L.tr[2 * t] + BUF * 8192), lds_tr(L.tr[2 * t + 1] + BUF * 8192));

  constexpr float SCALE = 0.08838834764831845f, NLOG2E = -1.4426950408889634f;
  unsigned pk = pack_bf16(pick(s0, e0), pick(s1, e1));
  pk = pack_bf16(__uint_as_float(pk << 16) * SCALE, __uint_as_float(pk & 0xffff0000u) * SCALE);
  const float a0 = __builtin_amdgcn_rcpf(1.0f + __builtin_amdgcn_exp2f(__uint_as_float(pk << 16) * NLOG2E));
  const float a1 = __builtin_amdgcn_rcpf(1.0f + __builtin_amdgcn_exp2f(__uint_as_float(pk & 0xffff0000u) * NLOG2E));
  pk = pack_bf16(a0, a1);
  const unsigned p0 = L.pw + (p_row(e0, L.g4) << 6), p1 = L.pw + 32 + (p_row(e1, L.g4) << 6);
  lds_st<unsigned short>(p0, (unsigned short)pk);
  lds_st<unsigned short>(p1, (unsigned short)(pk >> 16));
  const bf16x8 p = lds_ld<bf16x8>(L.pr);
  lds_st<unsigned short>(p0, 0);
  lds_st<unsigned short>(p1, 0);
#pragma unroll
  for (int t = 0; t < 8; ++t) acc[t] = mfma(xt[t], p, acc[t]);
}

__global__ __launch_bounds__(512, 2) void aggregate_kernel(
    const char* __restrict__ x, const char* __restrict__ wq, const char* __restrict__ wv,
    const unsigned* __restrict__ entries, const unsigned* __restrict__ offs, char* __restrict__ out,
    int N, int chunk) {
  __shared__ __attribute__((aligned(16))) char smem[L_TOTAL];
  const unsigned lds0 = (unsigned)(size_t)(LDS_AS char*)smem;
  const unsigned tid = threadIdx.x, lane = tid & 63;
  const unsigned w = __builtin_amdgcn_readfirstlane(tid >> 6);
  STAMP2(0);
  const unsigned bucket = (blockIdx.x & 7) * (NB / 8) + (blockIdx.x >> 3);
  const unsigned g = lane >> 4, c16 = lane & 15;
  const unsigned seg = tid >> 2, sj = tid & 3;
  const unsigned node0 = bucket * BN + w * TN;
  const unsigned ring = lds0 + L_RING + w * 16384;

  const u32x4 srd_x = make_srd(x - 3072, (unsigned)N * 256u + 3072u);
  const u32x4 srd_q = make_srd(wq, D * D * 2), srd_v = make_srd(wv, D * D * 2);
  const u32x4 srd_e = make_srd(entries, (unsigned)(NC * chunk + 64) * 4u);
  const unsigned o0 = 3072, o1 = 2048, o2 = 1024;

  unsigned ofs;
  asm volatile("global_load_dword %0, %1, %2" : "=v"(ofs) : "v"((seg * NB + bucket) * 4), "s"(offs) : "memory");

  Lane L;
#pragma unroll
  for (int q = 0; q < 4; ++q) L.dma[q] = (c16 ^ swz(4 * q + g)) << 4;
  {
    const unsigned r = (16 * w + g) * 256;
    dma4(srd_q, lds0 + L_RING + (w >> 1) * 16384 + 8192 + (w & 1) * 4096, r + L.dma[0], r + L.dma[1], r + L.dma[2], r + L.dma[3], 0, 0, 0, 0);
    const unsigned xr = (node0 + g) * 256;
    if (w < NW) dma4(srd_x, ring, xr + L.dma[0], xr + L.dma[1], xr + L.dma[2], xr + L.dma[3], o0, o0, o0, o0);
  }
  if (tid < 32) lds_st<unsigned>(lds0 + L_CNT + tid * 4, 0);
  if (w < NW) lds_st<u32x4>(lds0 + L_P + w * 1024 + lane * 16, u32x4{0, 0, 0, 0});
  bar();

  asm volatile("s_cmp_lt_u32 %1, 7\n\ts_cbranch_scc1 LW%=\n\ts_waitcnt vmcnt(4)\nLW%=:\n\ts_waitcnt vmcnt(8)"
               : "+v"(ofs) : "s"(w) : "memory", "scc");
  STAMP2(1);
  const unsigned seg_cnt = ofs >> 16;
  const unsigned seg_off = (seg * chunk + (ofs & 0xffff) + 4 * sj) * 4;

  u32x4 ea, eb;
  {
    const unsigned va = 4 * sj < seg_cnt ? seg_off : 0x7fff0000u;
    const unsigned vb = 16 + 4 * sj < seg_cnt ? seg_off + 64 : 0x7fff0000u;
    asm volatile("buffer_load_dwordx4 %0, %2, %4, 0 offen\n\tbuffer_load_dwordx4 %1, %3, %4, 0 offen"
                 : "=&v"(ea), "=&v"(eb) : "v"(va), "v"(vb), "s"(srd_e) : "memory");
  }
  {
    const unsigned r = (16 * w + g) * 256;
    dma4(srd_v, lds0 + L_WV + w * 4096, r + L.dma[0], r + L.dma[1], r + L.dma[2], r + L.dma[3], 0, 0, 0, 0);
  }

#pragma unroll
  for (int s = 0; s < 4; ++s) L.row[s] = ring + c16 * 256 + (((4 * s + g) ^ swz(c16)) << 4);
  const unsigned tq = c16 >> 2, tp = c16 & 3;
#pragma unroll
  for (int t = 0; t < 8; ++t)
#pragma unroll
    for (int h = 0; h < 2; ++h) {
      const unsigned row = 8 * g + 4 * h + tq;
      L.tr[2 * t + h] = ring + row * 256 + (((2 * t + (tp >> 1)) ^ swz(row & 15)) << 4) + 8 * (tp & 1);
    }
  L.pw = lds0 + L_P + w * 1024 + 2 * c16;
  L.pr = lds0 + L_P + w * 1024 + c16 * 64 + g * 16;
  L.g4 = 4 * g;

  f32x4 acc[8];
#pragma unroll
  for (int t = 0; t < 8; ++t) acc[t] = f32x4{0, 0, 0, 0};
  bf16x8 q[4];

  asm volatile("s_waitcnt vmcnt(4)" : "+v"(ea), "+v"(eb) :: "memory");
  STAMP2(2);
  bf16x8 xo[4];
  if (w < NW) {
#pragma unroll
    for (int s = 0; s < 4; ++s) xo[s] = lds_ld<bf16x8>(L.row[s]);
  }
  unsigned base = 0, take = 32;
  bool first = true;
  for (;;) {
    unsigned ent[8], rank[8];
#pragma unroll
    for (int j = 0; j < 4; ++j) { ent[j] = ea[j]; ent[4 + j] = eb[j]; }
    const unsigned lim = min(seg_cnt, base + take);
#pragma unroll
    for (int j = 0; j < 8; ++j) {
      const unsigned idx = base + (j >> 2) * 16 + 4 * sj + (j & 3);
      rank[j] = 0xffffffffu;
      if (idx < lim) rank[j] = __hip_atomic_fetch_add((LDS_AS unsigned*)(size_t)(lds0 + L_CNT + (ent[j] >> 27) * 4), 1u, __ATOMIC_RELAXED, __HIP_MEMORY_SCOPE_WORKGROUP);
    }
    if (seg_cnt > base + take) lds_st<unsigned>(lds0 + L_CNT + 31 * 4, 1);
    bar();
    STAMP2(3);
    const unsigned craw = lds_ld<unsigned>(lds0 + L_CNT + (lane & 31) * 4);
    const bool more = __builtin_amdgcn_readlane(craw, 31) != 0;
    const unsigned cnt = lane < NW * NPH ? craw : 0;
    unsigned inc = cnt;
    {
      const unsigned u = (unsigned)__builtin_amdgcn_update_dpp(0, (int)inc, 0x90, 0xf, 0xf, false);
      inc += tp >= 1 ? u : 0;
      const unsigned v = (unsigned)__builtin_amdgcn_update_dpp(0, (int)inc, 0x40, 0xf, 0xf, false);
      inc += tp >= 2 ? v : 0;
    }
    const unsigned tot = (unsigned)__builtin_amdgcn_update_dpp(0, (int)inc, 0xff, 0xf, 0xf, false);
    const unsigned pad = (tot + 31) & ~31u;
    unsigned sc = pad;
    sc = dpp_add<0x114>(sc);
    sc = dpp_add<0x118>(sc);
    sc = dpp_add<0x142, 0xa>(sc);
    const unsigned tile_start = sc - pad;
    unsigned sum = tot;
    sum = dpp_add<0x114>(sum);
    sum = dpp_add<0x118>(sum);
    sum = dpp_add<0x142, 0xa>(sum);
    if (__builtin_amdgcn_readlane(sum, 31) > LIST_CAP) {
      take = 16;
      bar();
      if (tid < 32) lds_st<unsigned>(lds0 + L_CNT + tid * 4, 0);
      bar();
      continue;
    }
    const unsigned bin_start = tile_start + inc - cnt;
#pragma unroll
    for (int j = 0; j < 8; ++j) {
      const unsigned st = __builtin_amdgcn_ds_bpermute((ent[j] >> 27) * 4, bin_start);
      if (rank[j] != 0xffffffffu) lds_st<unsigned>(lds0 + L_LIST + (st + rank[j]) * 4, ent[j]);
    }
    const unsigned wl = min(w, (unsigned)NW - 1) * NPH;
    const unsigned my_start = __builtin_amdgcn_readlane(tile_start, wl);
    const unsigned my_cnt = w < NW ? __builtin_amdgcn_readlane(tot, wl) : 0;
    const unsigned iters = (my_cnt + 31) >> 5;
    if (my_cnt + lane < iters * 32) lds_st<unsigned>(lds0 + L_LIST + (my_start + my_cnt + lane) * 4, SENTINEL);
    bar();

    STAMP2(4);
    unsigned la = lds0 + L_LIST + (my_start + 8 * g) * 4;
    unsigned le = lds0 + L_LIST + (my_start + 8 * tp + tq) * 4;
    if (iters > 0) issue_rows(L, srd_x, la, ring, o0, o1, o2);

    if (first) {
      first = false;
      if (w < NW) {
        const unsigned wbase = lds0 + L_RING + 8192 - ring;
#pragma unroll
        for (int s = 0; s < 4; ++s) {
          f32x4 qa = {0, 0, 0, 0}, qb = {0, 0, 0, 0};
#pragma unroll
          for (int k = 0; k < 4; ++k) {
            qa = mfma(join(lds_tr(L.tr[4 * s] + wbase + k * 16384), lds_tr(L.tr[4 * s + 1] + wbase + k * 16384)), xo[k], qa);
            qb = mfma(join(lds_tr(L.tr[4 * s + 2] + wbase + k * 16384), lds_tr(L.tr[4 * s + 3] + wbase + k * 16384)), xo[k], qb);
          }
          u32x4 f;
#pragma unroll
          for (int i = 0; i < 2; ++i) {
            const unsigned a = pack_bf16(qa[2 * i], qa[2 * i + 1]), b = pack_bf16(qb[2 * i], qb[2 * i + 1]);
#if defined(__HIP_DEVICE_COMPILE__)
            const auto r = __builtin_amdgcn_permlane32_swap(a, b, false, false);
            const auto r2 = __builtin_amdgcn_permlane16_swap(r[0], r[1], false, false);
            f[i] = r2[0];
            f[2 + i] = r2[1];
#endif
          }
          q[s] = __builtin_bit_cast(bf16x8, f);
        }
      }
      STAMP2(5);
      if (iters > 0) wait_vm<8>();
      else wait_vm<0>();
      bar();
    }

    STAMP2(6);
    for (unsigned it = 0; it < iters; it += 2) {
      if (it + 1 < iters) { issue_rows(L, srd_x, la + 128, ring + 8192, o0, o1, o2); wait_vm<8>(); }
      else wait_vm<0>();
      if (it == 0) STAMP2(7);
      edge_tile<0>(L, q, acc, le);
      if (it + 1 >= iters) break;
      if (it + 2 < iters) { issue_rows(L, srd_x, la + 256, ring, o0, o1, o2); wait_vm<8>(); }
      else wait_vm<0>();
      edge_tile<1>(L, q, acc, le + 128);
      la += 256;
      le += 256;
    }
    STAMP2(8);
    if (!more) break;

    base += take;
    bar();
    if (tid < 32) lds_st<unsigned>(lds0 + L_CNT + tid * 4, 0);
    const unsigned va = base + 4 * sj < seg_cnt ? seg_off + base * 4 : 0x7fff0000u;
    const unsigned vb = base + 16 + 4 * sj < seg_cnt ? seg_off + base * 4 + 64 : 0x7fff0000u;
    asm volatile("buffer_load_dwordx4 %0, %2, %4, 0 offen\n\tbuffer_load_dwordx4 %1, %3, %4, 0 offen\n\ts_waitcnt vmcnt(0)"
                 : "=&v"(ea), "=&v"(eb) : "v"(va), "v"(vb), "s"(srd_e) : "memory");
    bar();
  }

  if (w >= NW) return;
  bf16x8 ag[4];
#pragma unroll
  for (int s = 0; s < 4; ++s)
    ag[s] = __builtin_bit_cast(bf16x8, u32x4{pack_bf16(acc[2 * s][0], acc[2 * s][1]), pack_bf16(acc[2 * s][2], acc[2 * s][3]),
                                             pack_bf16(acc[2 * s + 1][0], acc[2 * s + 1][1]), pack_bf16(acc[2 * s + 1][2], acc[2 * s + 1][3])});
  const unsigned vrow = 4 * g + tq;
#pragma unroll
  for (int t = 0; t < 8; ++t) {
    const unsigned a = lds0 + L_WV + vrow * 256 + (((2 * t + (tp >> 1)) ^ swz(vrow)) << 4) + 8 * (tp & 1);
    f32x4 o = {0, 0, 0, 0};
#pragma unroll
    for (int s = 0; s < 4; ++s) o = mfma(join(lds_tr(a + s * 8192), lds_tr(a + s * 8192 + 4096)), ag[s], o);
    lds_st<u32x2>(ring + c16 * 256 + (((2 * t + (g >> 1)) ^ c16) << 4) + 8 * (g & 1),
                  u32x2{pack_bf16(o[0], o[1]), pack_bf16(o[2], o[3])});
  }
#pragma unroll
  for (int i = 0; i < 4; ++i) {
    const unsigned row = 4 * i + g;
    const u32x4 v = lds_ld<u32x4>(ring + row * 256 + ((c16 ^ row) << 4));
    if (row < TN && node0 + row < (unsigned)N) *(u32x4*)(out + (size_t)(node0 + row) * 256 + c16 * 16) = v;
  }
}

}

extern "C" void kernel_launch(void* const* d_in, const int* in_sizes, int n_in, void* d_out, int out_size,
                              void* d_ws, size_t ws_size, hipStream_t stream) {
  const char* x = (const char*)d_in[0];
  const int* src = (const int*)d_in[1];
  const int* dst = (const int*)d_in[2];
  const char* wq = (const char*)d_in[3];
  const char* wv = (const char*)d_in[4];
  const int N = in_sizes[0] / D, E = in_sizes[1];
  const int chunk = ((E + NC - 1) / NC + 3) & ~3;
  unsigned* offs = (unsigned*)d_ws;
  unsigned* entries = offs + NC * NB;
  const int warm_rows = (int)(N * 0.4);
  const unsigned phase_mul = (unsigned)((((unsigned long long)NPH) << 32) / (unsigned)N);
  partition_kernel<<<NC + 128, 1024, 0, stream>>>(src, dst, x, entries, offs, E, N, chunk, warm_rows, phase_mul);
  aggregate_kernel<<<NB, 512, 0, stream>>>(x, wq, wv, entries, offs, (char*)d_out, N, chunk);
}
```

```cpp
#include <hip/hip_runtime.h>
#include <hip/hip_bf16.h>

namespace {

typedef unsigned u32x2 __attribute__((ext_vector_type(2)));
typedef unsigned u32x4 __attribute__((ext_vector_type(4)));
typedef float f32x32 __attribute__((ext_vector_type(32)));
typedef unsigned u32x8 __attribute__((ext_vector_type(8)));
typedef unsigned u32x16 __attribute__((ext_vector_type(16)));
typedef unsigned u32x32 __attribute__((ext_vector_type(32)));
#define LDS_AS __attribute__((address_space(3)))
#define GLB_AS __attribute__((address_space(1)))
#define DI __device__ __forceinline__
#define SCHED_FENCE() __builtin_amdgcn_sched_barrier(0)

constexpr int D = 128;
constexpr int NW = 7;
constexpr int BN = 98;
constexpr int NB = 256;
constexpr int NC = 224;
constexpr int CHUNK_MAX = NW * 64 * 4;
constexpr int LO_CAP = 208;
constexpr int HI_CAP = 320;
constexpr int SEG_FAST = 64;
constexpr unsigned SRC_MASK = 0x00FFFF00u;
constexpr unsigned LIST_MASK = 0x03FFFFF0u;
constexpr unsigned SENTINEL = 0xFFFFFFF0u;
constexpr unsigned OOB = 0x7FFF0000u;
constexpr unsigned TAG_STEP = 0x9E3779B9u;

constexpr unsigned W_GRAN = 4096;
constexpr unsigned W_ENT = W_GRAN + (NC + 1) * NB * 8;

constexpr unsigned L_CNT = 0;
constexpr unsigned L_JUNK = 256;
constexpr unsigned L_LIST = 512;
constexpr unsigned TILE_BYTES = (LO_CAP + HI_CAP) * 4;
constexpr unsigned L_RING = 16384;
constexpr unsigned L_WV = L_RING + NW * 16384;
constexpr unsigned L_TOTAL = L_WV + 32768;
constexpr unsigned L_SORT = L_LIST;
constexpr unsigned L_HIST = L_SORT + CHUNK_MAX * 4;
constexpr unsigned L_LUT = L_HIST + 1024;
constexpr unsigned L_START = L_LUT + 512;
constexpr unsigned L_WQ = L_RING;
constexpr unsigned L_XO = L_RING + 32768;
constexpr unsigned CODE_BYTES = 12288;
static_assert(L_LUT == L_HIST + NB * 4 && L_TOTAL == 163840 && L_START + 1024 <= L_RING && L_LIST + NW * TILE_BYTES <= L_RING, "LDS budget");
static_assert(L_LIST + (NC * 8 + NW * 32) * 4 <= L_RING, "slow-path list");
constexpr int PF2 = 1;
constexpr int PF3 = 3;
constexpr int PFD = 0;
constexpr int LOX = 10;
constexpr int WQ_SLEEP = 5;
constexpr int XO_SLEEP = 10;
constexpr int CODE_WGS = 16;
constexpr unsigned GROUP_ROWS = BN * NB / 8;
constexpr unsigned WARM_ROWS = GROUP_ROWS + LOX * 1024;

#define TILE_SIZES 12, 12, 12, 16, 16, 15, 15
constexpr unsigned long long tile_table() {
  constexpr int n[NW] = {TILE_SIZES};
  unsigned long long v = 0, at = 0;
  for (int t = 0; t < NW; ++t) { at += n[t]; v |= at << (8 * (t + 1)); }
  return v;
}
constexpr unsigned long long TILE_TABLE = tile_table();
static_assert((TILE_TABLE >> 56) == BN, "tile sizes");
DI unsigned tile_start(unsigned t) { return (unsigned)(TILE_TABLE >> (8 * t)) & 255; }

__host__ __device__ constexpr unsigned tile_of(unsigned k) { return (k * 43 < k * 33 + 348 ? k * 43 : k * 33 + 348) >> 9; }
constexpr bool tile_of_ok() {
  for (unsigned k = 0; k < BN; ++k)
    if (k < ((TILE_TABLE >> (8 * tile_of(k))) & 255) || k >= ((TILE_TABLE >> (8 * tile_of(k) + 8)) & 255)) return false;
  return true;
}
static_assert(tile_of_ok(), "tile_of must match the tile sizes");

DI unsigned swz(unsigned row) {
  const unsigned b3 = (row >> 3) & 1, b2 = (row >> 2) & 1;
  return ((b3 ^ b2) << 3) | ((row & 3) << 1) | b2;
}
template <typename T> DI T lds_ld(unsigned addr) { return *(const LDS_AS T*)(size_t)addr; }
template <typename T> DI void lds_st(unsigned addr, T v) { *(LDS_AS T*)(size_t)addr = v; }
DI unsigned lds_add(unsigned addr, unsigned v) {
  return __hip_atomic_fetch_add((LDS_AS unsigned*)(size_t)addr, v, __ATOMIC_RELAXED, __HIP_MEMORY_SCOPE_WORKGROUP);
}
DI u32x4 make_srd(const void* p, unsigned bytes) {
  const unsigned long long a = (unsigned long long)p;
  return u32x4{(unsigned)a, (unsigned)(a >> 32) & 0xffffu, bytes, 0x00020000u};
}
DI void dma4(u32x4 srd, unsigned lds, unsigned v0, unsigned v1, unsigned v2, unsigned v3,
             unsigned o0, unsigned o1, unsigned o2, unsigned o3) {
  asm volatile(
      "s_mov_b32 m0, %5\n\t"
      "s_nop 0\n\t"
      "buffer_load_dwordx4 %0, %4, %6 offen lds\n\t"
      "buffer_load_dwordx4 %1, %4, %7 offen offset:1024 lds\n\t"
      "buffer_load_dwordx4 %2, %4, %8 offen offset:2048 lds\n\t"
      "buffer_load_dwordx4 %3, %4, %9 offen offset:3072 lds"
      :: "v"(v0), "v"(v1), "v"(v2), "v"(v3), "s"(srd), "s"(lds), "s"(o0), "s"(o1), "s"(o2), "s"(o3)
      : "memory");
}
template <int N> DI void wait_vm() { asm volatile("s_waitcnt vmcnt(%0)" :: "i"(N) : "memory"); }
DI void bar() { asm volatile("s_waitcnt lgkmcnt(0)\n\ts_barrier" ::: "memory"); }
template <int CTRL, int RMASK = 0xf> DI unsigned dpp_add(unsigned v) {
  return v + (unsigned)__builtin_amdgcn_update_dpp(0, (int)v, CTRL, RMASK, 0xf, false);
}
DI unsigned mad24(unsigned a, int b, unsigned c) {
  unsigned r;
  asm("v_mad_i32_i24 %0, %1, %2, %3" : "=v"(r) : "v"(a), "s"(b), "v"(c));
  return r;
}
template <typename T> DI void pin(T& v) { asm volatile("" : "+v"(v)); }

struct Dma { u32x4 srd; unsigned o0, o1, o2, o3; };

#define EDGE_LOOP_ASM \
  "s_cmp_lt_i32 %[left], 3\n" \
  "s_cbranch_scc1 LT0_%=\n" \
  "s_setprio 3\n" \
  "LL_%=:\n" \
  "s_waitcnt vmcnt(8)\n" \
  "ds_read_b128 v[80:83], v48 offset:0\n" \
  "ds_read_b128 v[84:87], v48 offset:4096\n" \
  "ds_read_b128 v[88:91], v49 offset:0\n" \
  "ds_read_b128 v[92:95], v49 offset:4096\n" \
  "v_cvt_pk_bf16_f32 v176, v180, v181\n" \
  "v_cvt_pk_bf16_f32 v177, v182, v183\n" \
  "ds_read_b128 v[96:99], v50 offset:0\n" \
  "ds_read_b128 v[100:103], v50 offset:4096\n" \
  "ds_read_b128 v[104:107], v51 offset:0\n" \
  "ds_read_b128 v[108:111], v51 offset:4096\n" \
  "v_cvt_pk_bf16_f32 v178, v184, v185\n" \
  "v_cvt_pk_bf16_f32 v179, v186, v187\n" \
  "ds_read2_b32 v[208:209], v68 offset0:0 offset1:16\n" \
  "ds_read_b128 v[200:203], v67 offset:256\n" \
  "s_waitcnt lgkmcnt(8)\n" \
  "v_mfma_f32_16x16x32_bf16 v[192:195], v[32:35], v[80:83], 0\n" \
  "ds_read_b64_tr_b16 v[112:113], v52 offset:0\n" \
  "v_mfma_f32_16x16x32_bf16 v[196:199], v[32:35], v[84:87], 0\n" \
  "ds_read_b64_tr_b16 v[116:117], v53 offset:0\n" \
  "s_waitcnt lgkmcnt(8)\n" \
  "v_mfma_f32_16x16x32_bf16 v[192:195], v[36:39], v[88:91], v[192:195]\n" \
  "ds_read_b64_tr_b16 v[120:121], v54 offset:0\n" \
  "v_mfma_f32_16x16x32_bf16 v[196:199], v[36:39], v[92:95], v[196:199]\n" \
  "ds_read_b64_tr_b16 v[124:125], v55 offset:0\n" \
  "s_waitcnt lgkmcnt(8)\n" \
  "v_mfma_f32_16x16x32_bf16 v[192:195], v[40:43], v[96:99], v[192:195]\n" \
  "ds_read_b64_tr_b16 v[128:129], v56 offset:0\n" \
  "v_mfma_f32_16x16x32_bf16 v[196:199], v[40:43], v[100:103], v[196:199]\n" \
  "ds_read_b64_tr_b16 v[132:133], v57 offset:0\n" \
  "s_waitcnt lgkmcnt(6)\n" \
  "v_mfma_f32_16x16x32_bf16 v[192:195], v[44:47], v[104:107], v[192:195]\n" \
  "ds_read_b64_tr_b16 v[136:137], v58 offset:0\n" \
  "v_mfma_f32_16x16x32_bf16 v[196:199], v[44:47], v[108:111], v[196:199]\n" \
  "ds_read_b64_tr_b16 v[140:141], v59 offset:0\n" \
  "v_mfma_f32_16x16x32_bf16 v[0:3], v[144:147], v[176:179], v[0:3]\n" \
  "ds_read_b128 v[204:207], v67 offset:320\n" \
  "ds_read_b64_tr_b16 v[114:115], v52 offset:4096\n" \
  "v_mfma_f32_16x16x32_bf16 v[4:7], v[148:151], v[176:179], v[4:7]\n" \
  "ds_read_b64_tr_b16 v[118:119], v53 offset:4096\n" \
  "ds_read_b64_tr_b16 v[122:123], v54 offset:4096\n" \
  "v_cmp_eq_u32_sdwa vcc, v208, v66 src0_sel:BYTE_3 src1_sel:DWORD\n" \
  "v_cvt_pk_bf16_f32 v210, v192, v193\n" \
  "v_cvt_pk_bf16_f32 v211, v194, v195\n" \
  "v_mfma_f32_16x16x32_bf16 v[8:11], v[152:155], v[176:179], v[8:11]\n" \
  "ds_read_b64_tr_b16 v[126:127], v55 offset:4096\n" \
  "ds_read_b64_tr_b16 v[130:131], v56 offset:4096\n" \
  "v_cvt_pk_bf16_f32 v212, v196, v197\n" \
  "v_cvt_pk_bf16_f32 v213, v198, v199\n" \
  "v_mfma_f32_16x16x32_bf16 v[12:15], v[156:159], v[176:179], v[12:15]\n" \
  "ds_read_b64_tr_b16 v[134:135], v57 offset:4096\n" \
  "ds_read_b64_tr_b16 v[138:139], v58 offset:4096\n" \
  "v_lshrrev_b64 v[210:211], v208, v[210:211]\n" \
  "v_lshrrev_b64 v[212:213], v209, v[212:213]\n" \
  "v_mfma_f32_16x16x32_bf16 v[16:19], v[160:163], v[176:179], v[16:19]\n" \
  "ds_read_b64_tr_b16 v[142:143], v59 offset:4096\n" \
  "s_add_u32 m0, %[ring], 0\n" \
  "v_and_or_b32 v200, v200, %[msk], v60\n" \
  "s_waitcnt lgkmcnt(8)\n" \
  "buffer_load_dwordx4 v200, %[srd], %[o0] offen lds\n" \
  "v_lshlrev_b32 v218, 16, v210\n" \
  "v_lshlrev_b32 v219, 16, v212\n" \
  "v_mfma_f32_16x16x32_bf16 v[20:23], v[164:167], v[176:179], v[20:23]\n" \
  "v_and_or_b32 v201, v201, %[msk], v61\n" \
  "v_mul_f32 v218, 0xbe0293ee, v218\n" \
  "v_mul_f32 v219, 0xbe0293ee, v219\n" \
  "buffer_load_dwordx4 v201, %[srd], %[o1] offen offset:1024 lds\n" \
  "v_exp_f32 v218, v218\n" \
  "v_exp_f32 v219, v219\n" \
  "v_mfma_f32_16x16x32_bf16 v[24:27], v[168:171], v[176:179], v[24:27]\n" \
  "v_and_or_b32 v202, v202, %[msk], v62\n" \
  "v_add_f32 v218, 1.0, v218\n" \
  "v_add_f32 v219, 1.0, v219\n" \
  "buffer_load_dwordx4 v202, %[srd], %[o2] offen offset:2048 lds\n" \
  "v_rcp_f32 v218, v218\n" \
  "v_rcp_f32 v219, v219\n" \
  "v_mfma_f32_16x16x32_bf16 v[28:31], v[172:175], v[176:179], v[28:31]\n" \
  "v_and_or_b32 v203, v203, %[msk], v63\n" \
  "v_cvt_pk_bf16_f32 v218, v218, v219\n" \
  "buffer_load_dwordx4 v203, %[srd], %[o3] offen offset:3072 lds\n" \
  "v_cndmask_b32_sdwa v188, v189, v218, vcc dst_sel:DWORD dst_unused:UNUSED_PAD src0_sel:DWORD src1_sel:WORD_0\n" \
  "v_cmp_eq_u32_sdwa vcc, v209, v66 src0_sel:BYTE_3 src1_sel:DWORD\n" \
  "s_waitcnt lgkmcnt(0)\n" \
  "s_add_u32 m0, %[ring], 4096\n" \
  "v_lshlrev_b64 v[214:215], v208, v[188:189]\n" \
  "v_cndmask_b32_sdwa v190, v191, v218, vcc dst_sel:DWORD dst_unused:UNUSED_PAD src0_sel:DWORD src1_sel:WORD_1\n" \
  "v_and_or_b32 v204, v204, %[msk], v60\n" \
  "v_lshlrev_b64 v[216:217], v209, v[190:191]\n" \
  "buffer_load_dwordx4 v204, %[srd], %[o0] offen lds\n" \
  "v_mfma_f32_16x16x16_bf16 v[180:183], v[214:215], v[64:65], 0\n" \
  "v_and_or_b32 v205, v205, %[msk], v61\n" \
  "v_mfma_f32_16x16x16_bf16 v[184:187], v[216:217], v[64:65], 0\n" \
  "buffer_load_dwordx4 v205, %[srd], %[o1] offen offset:1024 lds\n" \
  "v_and_or_b32 v206, v206, %[msk], v62\n" \
  "buffer_load_dwordx4 v206, %[srd], %[o2] offen offset:2048 lds\n" \
  "v_and_or_b32 v207, v207, %[msk], v63\n" \
  "s_cmp_lt_i32 %[left], 4\n" \
  "buffer_load_dwordx4 v207, %[srd], %[o3] offen offset:3072 lds\n" \
  "s_cbranch_scc1 LT1_%=\n" \
  "s_waitcnt vmcnt(8)\n" \
  "ds_read_b128 v[80:83], v48 offset:8192\n" \
  "ds_read_b128 v[84:87], v48 offset:12288\n" \
  "ds_read_b128 v[88:91], v49 offset:8192\n" \
  "ds_read_b128 v[92:95], v49 offset:12288\n" \
  "v_cvt_pk_bf16_f32 v176, v180, v181\n" \
  "v_cvt_pk_bf16_f32 v177, v182, v183\n" \
  "ds_read_b128 v[96:99], v50 offset:8192\n" \
  "ds_read_b128 v[100:103], v50 offset:12288\n" \
  "ds_read_b128 v[104:107], v51 offset:8192\n" \
  "ds_read_b128 v[108:111], v51 offset:12288\n" \
  "v_cvt_pk_bf16_f32 v178, v184, v185\n" \
  "v_cvt_pk_bf16_f32 v179, v186, v187\n" \
  "ds_read2_b32 v[208:209], v68 offset0:32 offset1:48\n" \
  "ds_read_b128 v[200:203], v67 offset:384\n" \
  "s_waitcnt lgkmcnt(8)\n" \
  "v_mfma_f32_16x16x32_bf16 v[192:195], v[32:35], v[80:83], 0\n" \
  "ds_read_b64_tr_b16 v[144:145], v52 offset:8192\n" \
  "v_mfma_f32_16x16x32_bf16 v[196:199], v[32:35], v[84:87], 0\n" \
  "ds_read_b64_tr_b16 v[148:149], v53 offset:8192\n" \
  "s_waitcnt lgkmcnt(8)\n" \
  "v_mfma_f32_16x16x32_bf16 v[192:195], v[36:39], v[88:91], v[192:195]\n" \
  "ds_read_b64_tr_b16 v[152:153], v54 offset:8192\n" \
  "v_mfma_f32_16x16x32_bf16 v[196:199], v[36:39], v[92:95], v[196:199]\n" \
  "ds_read_b64_tr_b16 v[156:157], v55 offset:8192\n" \
  "s_waitcnt lgkmcnt(8)\n" \
  "v_mfma_f32_16x16x32_bf16 v[192:195], v[40:43], v[96:99], v[192:195]\n" \
  "ds_read_b64_tr_b16 v[160:161], v56 offset:8192\n" \
  "v_mfma_f32_16x16x32_bf16 v[196:199], v[40:43], v[100:103], v[196:199]\n" \
  "ds_read_b64_tr_b16 v[164:165], v57 offset:8192\n" \
  "s_waitcnt lgkmcnt(6)\n" \
  "v_mfma_f32_16x16x32_bf16 v[192:195], v[44:47], v[104:107], v[192:195]\n" \
  "ds_read_b64_tr_b16 v[168:169], v58 offset:8192\n" \
  "v_mfma_f32_16x16x32_bf16 v[196:199], v[44:47], v[108:111], v[196:199]\n" \
  "ds_read_b64_tr_b16 v[172:173], v59 offset:8192\n" \
  "v_mfma_f32_16x16x32_bf16 v[0:3], v[112:115], v[176:179], v[0:3]\n" \
  "ds_read_b128 v[204:207], v67 offset:448\n" \
  "ds_read_b64_tr_b16 v[146:147], v52 offset:12288\n" \
  "v_mfma_f32_16x16x32_bf16 v[4:7], v[116:119], v[176:179], v[4:7]\n" \
  "ds_read_b64_tr_b16 v[150:151], v53 offset:12288\n" \
  "ds_read_b64_tr_b16 v[154:155], v54 offset:12288\n" \
  "v_cmp_eq_u32_sdwa vcc, v208, v66 src0_sel:BYTE_3 src1_sel:DWORD\n" \
  "v_cvt_pk_bf16_f32 v210, v192, v193\n" \
  "v_cvt_pk_bf16_f32 v211, v194, v195\n" \
  "v_mfma_f32_16x16x32_bf16 v[8:11], v[120:123], v[176:179], v[8:11]\n" \
  "ds_read_b64_tr_b16 v[158:159], v55 offset:12288\n" \
  "ds_read_b64_tr_b16 v[162:163], v56 offset:12288\n" \
  "v_cvt_pk_bf16_f32 v212, v196, v197\n" \
  "v_cvt_pk_bf16_f32 v213, v198, v199\n" \
  "v_mfma_f32_16x16x32_bf16 v[12:15], v[124:127], v[176:179], v[12:15]\n" \
  "ds_read_b64_tr_b16 v[166:167], v57 offset:12288\n" \
  "ds_read_b64_tr_b16 v[170:171], v58 offset:12288\n" \
  "v_lshrrev_b64 v[210:211], v208, v[210:211]\n" \
  "v_lshrrev_b64 v[212:213], v209, v[212:213]\n" \
  "v_mfma_f32_16x16x32_bf16 v[16:19], v[128:131], v[176:179], v[16:19]\n" \
  "ds_read_b64_tr_b16 v[174:175], v59 offset:12288\n" \
  "s_add_u32 m0, %[ring], 8192\n" \
  "v_and_or_b32 v200, v200, %[msk], v60\n" \
  "s_waitcnt lgkmcnt(8)\n" \
  "buffer_load_dwordx4 v200, %[srd], %[o0] offen lds\n" \
  "v_lshlrev_b32 v218, 16, v210\n" \
  "v_lshlrev_b32 v219, 16, v212\n" \
  "v_mfma_f32_16x16x32_bf16 v[20:23], v[132:135], v[176:179], v[20:23]\n" \
  "v_and_or_b32 v201, v201, %[msk], v61\n" \
  "v_mul_f32 v218, 0xbe0293ee, v218\n" \
  "v_mul_f32 v219, 0xbe0293ee, v219\n" \
  "buffer_load_dwordx4 v201, %[srd], %[o1] offen offset:1024 lds\n" \
  "v_exp_f32 v218, v218\n" \
  "v_exp_f32 v219, v219\n" \
  "v_mfma_f32_16x16x32_bf16 v[24:27], v[136:139], v[176:179], v[24:27]\n" \
  "v_and_or_b32 v202, v202, %[msk], v62\n" \
  "v_add_f32 v218, 1.0, v218\n" \
  "v_add_f32 v219, 1.0, v219\n" \
  "buffer_load_dwordx4 v202, %[srd], %[o2] offen offset:2048 lds\n" \
  "v_rcp_f32 v218, v218\n" \
  "v_rcp_f32 v219, v219\n" \
  "v_mfma_f32_16x16x32_bf16 v[28:31], v[140:143], v[176:179], v[28:31]\n" \
  "v_and_or_b32 v203, v203, %[msk], v63\n" \
  "v_cvt_pk_bf16_f32 v218, v218, v219\n" \
  "buffer_load_dwordx4 v203, %[srd], %[o3] offen offset:3072 lds\n" \
  "v_cndmask_b32_sdwa v188, v189, v218, vcc dst_sel:DWORD dst_unused:UNUSED_PAD src0_sel:DWORD src1_sel:WORD_0\n" \
  "v_cmp_eq_u32_sdwa vcc, v209, v66 src0_sel:BYTE_3 src1_sel:DWORD\n" \
  "s_waitcnt lgkmcnt(0)\n" \
  "s_add_u32 m0, %[ring], 12288\n" \
  "v_lshlrev_b64 v[214:215], v208, v[188:189]\n" \
  "v_cndmask_b32_sdwa v190, v191, v218, vcc dst_sel:DWORD dst_unused:UNUSED_PAD src0_sel:DWORD src1_sel:WORD_1\n" \
  "v_and_or_b32 v204, v204, %[msk], v60\n" \
  "v_lshlrev_b64 v[216:217], v209, v[190:191]\n" \
  "buffer_load_dwordx4 v204, %[srd], %[o0] offen lds\n" \
  "v_mfma_f32_16x16x16_bf16 v[180:183], v[214:215], v[64:65], 0\n" \
  "v_and_or_b32 v205, v205, %[msk], v61\n" \
  "v_mfma_f32_16x16x16_bf16 v[184:187], v[216:217], v[64:65], 0\n" \
  "buffer_load_dwordx4 v205, %[srd], %[o1] offen offset:1024 lds\n" \
  "v_and_or_b32 v206, v206, %[msk], v62\n" \
  "buffer_load_dwordx4 v206, %[srd], %[o2] offen offset:2048 lds\n" \
  "v_and_or_b32 v207, v207, %[msk], v63\n" \
  "s_sub_i32 %[left], %[left], 2\n" \
  "buffer_load_dwordx4 v207, %[srd], %[o3] offen offset:3072 lds\n" \
  "v_add_u32 v67, 0x100, v67\n" \
  "v_add_u32 v68, 0x100, v68\n" \
  "s_cmp_gt_i32 %[left], 6\n" \
  "s_cbranch_scc1 LL_%=\n" \
  "s_setprio 2\n" \
  "s_cmp_gt_i32 %[left], 4\n" \
  "s_cbranch_scc1 LL_%=\n" \
  "s_setprio 1\n" \
  "s_cmp_gt_i32 %[left], 2\n" \
  "s_cbranch_scc1 LL_%=\n" \
  "LT0_%=:\n" \
  "s_setprio 0\n" \
  "s_cmp_gt_i32 %[left], 1\n" \
  "s_cbranch_scc1 LW0_%=\n" \
  "s_waitcnt vmcnt(0)\n" \
  "LW0_%=:\n" \
  "s_waitcnt vmcnt(8)\n" \
  "ds_read_b128 v[80:83], v48 offset:0\n" \
  "ds_read_b128 v[84:87], v48 offset:4096\n" \
  "ds_read_b128 v[88:91], v49 offset:0\n" \
  "ds_read_b128 v[92:95], v49 offset:4096\n" \
  "v_cvt_pk_bf16_f32 v176, v180, v181\n" \
  "v_cvt_pk_bf16_f32 v177, v182, v183\n" \
  "ds_read_b128 v[96:99], v50 offset:0\n" \
  "ds_read_b128 v[100:103], v50 offset:4096\n" \
  "ds_read_b128 v[104:107], v51 offset:0\n" \
  "ds_read_b128 v[108:111], v51 offset:4096\n" \
  "v_cvt_pk_bf16_f32 v178, v184, v185\n" \
  "v_cvt_pk_bf16_f32 v179, v186, v187\n" \
  "ds_read2_b32 v[208:209], v68 offset0:0 offset1:16\n" \
  "s_waitcnt lgkmcnt(7)\n" \
  "v_mfma_f32_16x16x32_bf16 v[192:195], v[32:35], v[80:83], 0\n" \
  "ds_read_b64_tr_b16 v[112:113], v52 offset:0\n" \
  "v_mfma_f32_16x16x32_bf16 v[196:199], v[32:35], v[84:87], 0\n" \
  "ds_read_b64_tr_b16 v[116:117], v53 offset:0\n" \
  "s_waitcnt lgkmcnt(7)\n" \
  "v_mfma_f32_16x16x32_bf16 v[192:195], v[36:39], v[88:91], v[192:195]\n" \
  "ds_read_b64_tr_b16 v[120:121], v54 offset:0\n" \
  "v_mfma_f32_16x16x32_bf16 v[196:199], v[36:39], v[92:95], v[196:199]\n" \
  "ds_read_b64_tr_b16 v[124:125], v55 offset:0\n" \
  "s_waitcnt lgkmcnt(7)\n" \
  "v_mfma_f32_16x16x32_bf16 v[192:195], v[40:43], v[96:99], v[192:195]\n" \
  "ds_read_b64_tr_b16 v[128:129], v56 offset:0\n" \
  "v_mfma_f32_16x16x32_bf16 v[196:199], v[40:43], v[100:103], v[196:199]\n" \
  "ds_read_b64_tr_b16 v[132:133], v57 offset:0\n" \
  "s_waitcnt lgkmcnt(6)\n" \
  "v_mfma_f32_16x16x32_bf16 v[192:195], v[44:47], v[104:107], v[192:195]\n" \
  "ds_read_b64_tr_b16 v[136:137], v58 offset:0\n" \
  "v_mfma_f32_16x16x32_bf16 v[196:199], v[44:47], v[108:111], v[196:199]\n" \
  "ds_read_b64_tr_b16 v[140:141], v59 offset:0\n" \
  "v_mfma_f32_16x16x32_bf16 v[0:3], v[144:147], v[176:179], v[0:3]\n" \
  "ds_read_b64_tr_b16 v[114:115], v52 offset:4096\n" \
  "v_mfma_f32_16x16x32_bf16 v[4:7], v[148:151], v[176:179], v[4:7]\n" \
  "ds_read_b64_tr_b16 v[118:119], v53 offset:4096\n" \
  "ds_read_b64_tr_b16 v[122:123], v54 offset:4096\n" \
  "v_cmp_eq_u32_sdwa vcc, v208, v66 src0_sel:BYTE_3 src1_sel:DWORD\n" \
  "s_nop 0\n" \
  "v_cvt_pk_bf16_f32 v210, v192, v193\n" \
  "v_cvt_pk_bf16_f32 v211, v194, v195\n" \
  "v_mfma_f32_16x16x32_bf16 v[8:11], v[152:155], v[176:179], v[8:11]\n" \
  "ds_read_b64_tr_b16 v[126:127], v55 offset:4096\n" \
  "ds_read_b64_tr_b16 v[130:131], v56 offset:4096\n" \
  "v_cvt_pk_bf16_f32 v212, v196, v197\n" \
  "v_cvt_pk_bf16_f32 v213, v198, v199\n" \
  "v_mfma_f32_16x16x32_bf16 v[12:15], v[156:159], v[176:179], v[12:15]\n" \
  "ds_read_b64_tr_b16 v[134:135], v57 offset:4096\n" \
  "ds_read_b64_tr_b16 v[138:139], v58 offset:4096\n" \
  "v_lshrrev_b64 v[210:211], v208, v[210:211]\n" \
  "v_lshrrev_b64 v[212:213], v209, v[212:213]\n" \
  "v_mfma_f32_16x16x32_bf16 v[16:19], v[160:163], v[176:179], v[16:19]\n" \
  "ds_read_b64_tr_b16 v[142:143], v59 offset:4096\n" \
  "v_lshlrev_b32 v218, 16, v210\n" \
  "v_lshlrev_b32 v219, 16, v212\n" \
  "v_mfma_f32_16x16x32_bf16 v[20:23], v[164:167], v[176:179], v[20:23]\n" \
  "v_mul_f32 v218, 0xbe0293ee, v218\n" \
  "v_mul_f32 v219, 0xbe0293ee, v219\n" \
  "v_exp_f32 v218, v218\n" \
  "v_exp_f32 v219, v219\n" \
  "v_mfma_f32_16x16x32_bf16 v[24:27], v[168:171], v[176:179], v[24:27]\n" \
  "v_add_f32 v218, 1.0, v218\n" \
  "v_add_f32 v219, 1.0, v219\n" \
  "v_rcp_f32 v218, v218\n" \
  "v_rcp_f32 v219, v219\n" \
  "v_mfma_f32_16x16x32_bf16 v[28:31], v[172:175], v[176:179], v[28:31]\n" \
  "v_cvt_pk_bf16_f32 v218, v218, v219\n" \
  "v_cndmask_b32_sdwa v188, v189, v218, vcc dst_sel:DWORD dst_unused:UNUSED_PAD src0_sel:DWORD src1_sel:WORD_0\n" \
  "v_cmp_eq_u32_sdwa vcc, v209, v66 src0_sel:BYTE_3 src1_sel:DWORD\n" \
  "s_nop 0\n" \
  "v_lshlrev_b64 v[214:215], v208, v[188:189]\n" \
  "v_cndmask_b32_sdwa v190, v191, v218, vcc dst_sel:DWORD dst_unused:UNUSED_PAD src0_sel:DWORD src1_sel:WORD_1\n" \
  "v_lshlrev_b64 v[216:217], v209, v[190:191]\n" \
  "v_mfma_f32_16x16x16_bf16 v[180:183], v[214:215], v[64:65], 0\n" \
  "s_nop 0\n" \
  "v_mfma_f32_16x16x16_bf16 v[184:187], v[216:217], v[64:65], 0\n" \
  "s_cbranch_scc0 LX0_%=\n" \
  "s_mov_b32 %[left], 2\n" \
  "LT1_%=:\n" \
  "s_setprio 0\n" \
  "s_cmp_gt_i32 %[left], 2\n" \
  "s_cbranch_scc1 LW1_%=\n" \
  "s_waitcnt vmcnt(0)\n" \
  "LW1_%=:\n" \
  "s_waitcnt vmcnt(8)\n" \
  "ds_read_b128 v[80:83], v48 offset:8192\n" \
  "ds_read_b128 v[84:87], v48 offset:12288\n" \
  "ds_read_b128 v[88:91], v49 offset:8192\n" \
  "ds_read_b128 v[92:95], v49 offset:12288\n" \
  "v_cvt_pk_bf16_f32 v176, v180, v181\n" \
  "v_cvt_pk_bf16_f32 v177, v182, v183\n" \
  "ds_read_b128 v[96:99], v50 offset:8192\n" \
  "ds_read_b128 v[100:103], v50 offset:12288\n" \
  "ds_read_b128 v[104:107], v51 offset:8192\n" \
  "ds_read_b128 v[108:111], v51 offset:12288\n" \
  "v_cvt_pk_bf16_f32 v178, v184, v185\n" \
  "v_cvt_pk_bf16_f32 v179, v186, v187\n" \
  "ds_read2_b32 v[208:209], v68 offset0:32 offset1:48\n" \
  "s_waitcnt lgkmcnt(7)\n" \
  "v_mfma_f32_16x16x32_bf16 v[192:195], v[32:35], v[80:83], 0\n" \
  "ds_read_b64_tr_b16 v[144:145], v52 offset:8192\n" \
  "v_mfma_f32_16x16x32_bf16 v[196:199], v[32:35], v[84:87], 0\n" \
  "ds_read_b64_tr_b16 v[148:149], v53 offset:8192\n" \
  "s_waitcnt lgkmcnt(7)\n" \
  "v_mfma_f32_16x16x32_bf16 v[192:195], v[36:39], v[88:91], v[192:195]\n" \
  "ds_read_b64_tr_b16 v[152:153], v54 offset:8192\n" \
  "v_mfma_f32_16x16x32_bf16 v[196:199], v[36:39], v[92:95], v[196:199]\n" \
  "ds_read_b64_tr_b16 v[156:157], v55 offset:8192\n" \
  "s_waitcnt lgkmcnt(7)\n" \
  "v_mfma_f32_16x16x32_bf16 v[192:195], v[40:43], v[96:99], v[192:195]\n" \
  "ds_read_b64_tr_b16 v[160:161], v56 offset:8192\n" \
  "v_mfma_f32_16x16x32_bf16 v[196:199], v[40:43], v[100:103], v[196:199]\n" \
  "ds_read_b64_tr_b16 v[164:165], v57 offset:8192\n" \
  "s_waitcnt lgkmcnt(6)\n" \
  "v_mfma_f32_16x16x32_bf16 v[192:195], v[44:47], v[104:107], v[192:195]\n" \
  "ds_read_b64_tr_b16 v[168:169], v58 offset:8192\n" \
  "v_mfma_f32_16x16x32_bf16 v[196:199], v[44:47], v[108:111], v[196:199]\n" \
  "ds_read_b64_tr_b16 v[172:173], v59 offset:8192\n" \
  "v_mfma_f32_16x16x32_bf16 v[0:3], v[112:115], v[176:179], v[0:3]\n" \
  "ds_read_b64_tr_b16 v[146:147], v52 offset:12288\n" \
  "v_mfma_f32_16x16x32_bf16 v[4:7], v[116:119], v[176:179], v[4:7]\n" \
  "ds_read_b64_tr_b16 v[150:151], v53 offset:12288\n" \
  "ds_read_b64_tr_b16 v[154:155], v54 offset:12288\n" \
  "v_cmp_eq_u32_sdwa vcc, v208, v66 src0_sel:BYTE_3 src1_sel:DWORD\n" \
  "s_nop 0\n" \
  "v_cvt_pk_bf16_f32 v210, v192, v193\n" \
  "v_cvt_pk_bf16_f32 v211, v194, v195\n" \
  "v_mfma_f32_16x16x32_bf16 v[8:11], v[120:123], v[176:179], v[8:11]\n" \
  "ds_read_b64_tr_b16 v[158:159], v55 offset:12288\n" \
  "ds_read_b64_tr_b16 v[162:163], v56 offset:12288\n" \
  "v_cvt_pk_bf16_f32 v212, v196, v197\n" \
  "v_cvt_pk_bf16_f32 v213, v198, v199\n" \
  "v_mfma_f32_16x16x32_bf16 v[12:15], v[124:127], v[176:179], v[12:15]\n" \
  "ds_read_b64_tr_b16 v[166:167], v57 offset:12288\n" \
  "ds_read_b64_tr_b16 v[170:171], v58 offset:12288\n" \
  "v_lshrrev_b64 v[210:211], v208, v[210:211]\n" \
  "v_lshrrev_b64 v[212:213], v209, v[212:213]\n" \
  "v_mfma_f32_16x16x32_bf16 v[16:19], v[128:131], v[176:179], v[16:19]\n" \
  "ds_read_b64_tr_b16 v[174:175], v59 offset:12288\n" \
  "v_lshlrev_b32 v218, 16, v210\n" \
  "v_lshlrev_b32 v219, 16, v212\n" \
  "v_mfma_f32_16x16x32_bf16 v[20:23], v[132:135], v[176:179], v[20:23]\n" \
  "v_mul_f32 v218, 0xbe0293ee, v218\n" \
  "v_mul_f32 v219, 0xbe0293ee, v219\n" \
  "v_exp_f32 v218, v218\n" \
  "v_exp_f32 v219, v219\n" \
  "v_mfma_f32_16x16x32_bf16 v[24:27], v[136:139], v[176:179], v[24:27]\n" \
  "v_add_f32 v218, 1.0, v218\n" \
  "v_add_f32 v219, 1.0, v219\n" \
  "v_rcp_f32 v218, v218\n" \
  "v_rcp_f32 v219, v219\n" \
  "v_mfma_f32_16x16x32_bf16 v[28:31], v[140:143], v[176:179], v[28:31]\n" \
  "v_cvt_pk_bf16_f32 v218, v218, v219\n" \
  "v_cndmask_b32_sdwa v188, v189, v218, vcc dst_sel:DWORD dst_unused:UNUSED_PAD src0_sel:DWORD src1_sel:WORD_0\n" \
  "v_cmp_eq_u32_sdwa vcc, v209, v66 src0_sel:BYTE_3 src1_sel:DWORD\n" \
  "s_nop 0\n" \
  "v_lshlrev_b64 v[214:215], v208, v[188:189]\n" \
  "v_cndmask_b32_sdwa v190, v191, v218, vcc dst_sel:DWORD dst_unused:UNUSED_PAD src0_sel:DWORD src1_sel:WORD_1\n" \
  "v_lshlrev_b64 v[216:217], v209, v[190:191]\n" \
  "v_mfma_f32_16x16x16_bf16 v[180:183], v[214:215], v[64:65], 0\n" \
  "s_nop 0\n" \
  "v_mfma_f32_16x16x16_bf16 v[184:187], v[216:217], v[64:65], 0\n" \
  "s_cbranch_scc0 LX1_%=\n" \
  "s_mov_b32 %[left], 1\n" \
  "v_add_u32 v67, 0x100, v67\n" \
  "v_add_u32 v68, 0x100, v68\n" \
  "s_branch LT0_%=\n" \
  "LX0_%=:\n" \
  "s_waitcnt lgkmcnt(0)\n" \
  "ds_read_b64_tr_b16 v[80:81], v70 offset:0\n" \
  "ds_read_b64_tr_b16 v[82:83], v70 offset:4096\n" \
  "ds_read_b64_tr_b16 v[84:85], v71 offset:0\n" \
  "ds_read_b64_tr_b16 v[86:87], v71 offset:4096\n" \
  "ds_read_b64_tr_b16 v[88:89], v72 offset:0\n" \
  "ds_read_b64_tr_b16 v[90:91], v72 offset:4096\n" \
  "s_cmp_lg_u64 %[more], 0\n" \
  "v_cvt_pk_bf16_f32 v176, v180, v181\n" \
  "v_cvt_pk_bf16_f32 v177, v182, v183\n" \
  "v_cvt_pk_bf16_f32 v178, v184, v185\n" \
  "v_cvt_pk_bf16_f32 v179, v186, v187\n" \
  "ds_read_b64_tr_b16 v[92:93], v73 offset:0\n" \
  "ds_read_b64_tr_b16 v[94:95], v73 offset:4096\n" \
  "v_mfma_f32_16x16x32_bf16 v[0:3], v[112:115], v[176:179], v[0:3]\n" \
  "ds_read_b64_tr_b16 v[96:97], v74 offset:0\n" \
  "ds_read_b64_tr_b16 v[98:99], v74 offset:4096\n" \
  "v_mfma_f32_16x16x32_bf16 v[4:7], v[116:119], v[176:179], v[4:7]\n" \
  "ds_read_b64_tr_b16 v[100:101], v75 offset:0\n" \
  "ds_read_b64_tr_b16 v[102:103], v75 offset:4096\n" \
  "v_mfma_f32_16x16x32_bf16 v[8:11], v[120:123], v[176:179], v[8:11]\n" \
  "ds_read_b64_tr_b16 v[104:105], v76 offset:0\n" \
  "ds_read_b64_tr_b16 v[106:107], v76 offset:4096\n" \
  "v_mfma_f32_16x16x32_bf16 v[12:15], v[124:127], v[176:179], v[12:15]\n" \
  "ds_read_b64_tr_b16 v[108:109], v77 offset:0\n" \
  "ds_read_b64_tr_b16 v[110:111], v77 offset:4096\n" \
  "v_mfma_f32_16x16x32_bf16 v[16:19], v[128:131], v[176:179], v[16:19]\n" \
  "v_cvt_pk_bf16_f32 v192, v0, v1\n" \
  "v_cvt_pk_bf16_f32 v193, v2, v3\n" \
  "v_mfma_f32_16x16x32_bf16 v[20:23], v[132:135], v[176:179], v[20:23]\n" \
  "v_cvt_pk_bf16_f32 v194, v4, v5\n" \
  "v_cvt_pk_bf16_f32 v195, v6, v7\n" \
  "v_mfma_f32_16x16x32_bf16 v[24:27], v[136:139], v[176:179], v[24:27]\n" \
  "v_cvt_pk_bf16_f32 v196, v8, v9\n" \
  "v_cvt_pk_bf16_f32 v197, v10, v11\n" \
  "v_mfma_f32_16x16x32_bf16 v[28:31], v[140:143], v[176:179], v[28:31]\n" \
  "v_cvt_pk_bf16_f32 v198, v12, v13\n" \
  "v_cvt_pk_bf16_f32 v199, v14, v15\n" \
  "s_branch LE_%=\n" \
  "LX1_%=:\n" \
  "s_waitcnt lgkmcnt(0)\n" \
  "ds_read_b64_tr_b16 v[80:81], v70 offset:0\n" \
  "ds_read_b64_tr_b16 v[82:83], v70 offset:4096\n" \
  "ds_read_b64_tr_b16 v[84:85], v71 offset:0\n" \
  "ds_read_b64_tr_b16 v[86:87], v71 offset:4096\n" \
  "ds_read_b64_tr_b16 v[88:89], v72 offset:0\n" \
  "ds_read_b64_tr_b16 v[90:91], v72 offset:4096\n" \
  "s_cmp_lg_u64 %[more], 0\n" \
  "v_cvt_pk_bf16_f32 v176, v180, v181\n" \
  "v_cvt_pk_bf16_f32 v177, v182, v183\n" \
  "v_cvt_pk_bf16_f32 v178, v184, v185\n" \
  "v_cvt_pk_bf16_f32 v179, v186, v187\n" \
  "ds_read_b64_tr_b16 v[92:93], v73 offset:0\n" \
  "ds_read_b64_tr_b16 v[94:95], v73 offset:4096\n" \
  "v_mfma_f32_16x16x32_bf16 v[0:3], v[144:147], v[176:179], v[0:3]\n" \
  "ds_read_b64_tr_b16 v[96:97], v74 offset:0\n" \
  "ds_read_b64_tr_b16 v[98:99], v74 offset:4096\n" \
  "v_mfma_f32_16x16x32_bf16 v[4:7], v[148:151], v[176:179], v[4:7]\n" \
  "ds_read_b64_tr_b16 v[100:101], v75 offset:0\n" \
  "ds_read_b64_tr_b16 v[102:103], v75 offset:4096\n" \
  "v_mfma_f32_16x16x32_bf16 v[8:11], v[152:155], v[176:179], v[8:11]\n" \
  "ds_read_b64_tr_b16 v[104:105], v76 offset:0\n" \
  "ds_read_b64_tr_b16 v[106:107], v76 offset:4096\n" \
  "v_mfma_f32_16x16x32_bf16 v[12:15], v[156:159], v[176:179], v[12:15]\n" \
  "ds_read_b64_tr_b16 v[108:109], v77 offset:0\n" \
  "ds_read_b64_tr_b16 v[110:111], v77 offset:4096\n" \
  "v_mfma_f32_16x16x32_bf16 v[16:19], v[160:163], v[176:179], v[16:19]\n" \
  "v_cvt_pk_bf16_f32 v192, v0, v1\n" \
  "v_cvt_pk_bf16_f32 v193, v2, v3\n" \
  "v_mfma_f32_16x16x32_bf16 v[20:23], v[164:167], v[176:179], v[20:23]\n" \
  "v_cvt_pk_bf16_f32 v194, v4, v5\n" \
  "v_cvt_pk_bf16_f32 v195, v6, v7\n" \
  "v_mfma_f32_16x16x32_bf16 v[24:27], v[168:171], v[176:179], v[24:27]\n" \
  "v_cvt_pk_bf16_f32 v196, v8, v9\n" \
  "v_cvt_pk_bf16_f32 v197, v10, v11\n" \
  "v_mfma_f32_16x16x32_bf16 v[28:31], v[172:175], v[176:179], v[28:31]\n" \
  "v_cvt_pk_bf16_f32 v198, v12, v13\n" \
  "v_cvt_pk_bf16_f32 v199, v14, v15\n" \
  "LE_%=:\n" \
  "s_cbranch_scc1 LZ_%=\n" \
  "ds_read_b64_tr_b16 v[112:113], v70 offset:8192\n" \
  "ds_read_b64_tr_b16 v[114:115], v70 offset:12288\n" \
  "s_waitcnt lgkmcnt(15)\n" \
  "v_mfma_f32_16x16x32_bf16 v[144:147], v[80:83], v[192:195], 0\n" \
  "ds_read_b64_tr_b16 v[116:117], v71 offset:8192\n" \
  "ds_read_b64_tr_b16 v[118:119], v71 offset:12288\n" \
  "v_cvt_pk_bf16_f32 v200, v16, v17\n" \
  "s_waitcnt lgkmcnt(15)\n" \
  "v_mfma_f32_16x16x32_bf16 v[148:151], v[84:87], v[192:195], 0\n" \
  "ds_read_b64_tr_b16 v[120:121], v72 offset:8192\n" \
  "ds_read_b64_tr_b16 v[122:123], v72 offset:12288\n" \
  "v_cvt_pk_bf16_f32 v201, v18, v19\n" \
  "s_waitcnt lgkmcnt(15)\n" \
  "v_mfma_f32_16x16x32_bf16 v[152:155], v[88:91], v[192:195], 0\n" \
  "ds_read_b64_tr_b16 v[124:125], v73 offset:8192\n" \
  "ds_read_b64_tr_b16 v[126:127], v73 offset:12288\n" \
  "v_cvt_pk_bf16_f32 v202, v20, v21\n" \
  "s_waitcnt lgkmcnt(15)\n" \
  "v_mfma_f32_16x16x32_bf16 v[156:159], v[92:95], v[192:195], 0\n" \
  "ds_read_b64_tr_b16 v[128:129], v74 offset:8192\n" \
  "ds_read_b64_tr_b16 v[130:131], v74 offset:12288\n" \
  "v_cvt_pk_bf16_f32 v203, v22, v23\n" \
  "s_waitcnt lgkmcnt(15)\n" \
  "v_mfma_f32_16x16x32_bf16 v[160:163], v[96:99], v[192:195], 0\n" \
  "ds_read_b64_tr_b16 v[132:133], v75 offset:8192\n" \
  "ds_read_b64_tr_b16 v[134:135], v75 offset:12288\n" \
  "v_cvt_pk_bf16_f32 v204, v24, v25\n" \
  "s_waitcnt lgkmcnt(15)\n" \
  "v_mfma_f32_16x16x32_bf16 v[164:167], v[100:103], v[192:195], 0\n" \
  "ds_read_b64_tr_b16 v[136:137], v76 offset:8192\n" \
  "ds_read_b64_tr_b16 v[138:139], v76 offset:12288\n" \
  "v_cvt_pk_bf16_f32 v205, v26, v27\n" \
  "s_waitcnt lgkmcnt(15)\n" \
  "v_mfma_f32_16x16x32_bf16 v[168:171], v[104:107], v[192:195], 0\n" \
  "ds_read_b64_tr_b16 v[140:141], v77 offset:8192\n" \
  "ds_read_b64_tr_b16 v[142:143], v77 offset:12288\n" \
  "v_cvt_pk_bf16_f32 v206, v28, v29\n" \
  "s_waitcnt lgkmcnt(15)\n" \
  "v_mfma_f32_16x16x32_bf16 v[172:175], v[108:111], v[192:195], 0\n" \
  "v_cvt_pk_bf16_f32 v207, v30, v31\n" \
  "s_waitcnt lgkmcnt(14)\n" \
  "v_mfma_f32_16x16x32_bf16 v[144:147], v[112:115], v[196:199], v[144:147]\n" \
  "ds_read_b64_tr_b16 v[80:81], v70 offset:16384\n" \
  "ds_read_b64_tr_b16 v[82:83], v70 offset:20480\n" \
  "s_waitcnt lgkmcnt(14)\n" \
  "v_mfma_f32_16x16x32_bf16 v[148:151], v[116:119], v[196:199], v[148:151]\n" \
  "ds_read_b64_tr_b16 v[84:85], v71 offset:16384\n" \
  "ds_read_b64_tr_b16 v[86:87], v71 offset:20480\n" \
  "s_waitcnt lgkmcnt(14)\n" \
  "v_mfma_f32_16x16x32_bf16 v[152:155], v[120:123], v[196:199], v[152:155]\n" \
  "ds_read_b64_tr_b16 v[88:89], v72 offset:16384\n" \
  "ds_read_b64_tr_b16 v[90:91], v72 offset:20480\n" \
  "s_waitcnt lgkmcnt(14)\n" \
  "v_mfma_f32_16x16x32_bf16 v[156:159], v[124:127], v[196:199], v[156:159]\n" \
  "ds_read_b64_tr_b16 v[92:93], v73 offset:16384\n" \
  "ds_read_b64_tr_b16 v[94:95], v73 offset:20480\n" \
  "s_waitcnt lgkmcnt(14)\n" \
  "v_mfma_f32_16x16x32_bf16 v[160:163], v[128:131], v[196:199], v[160:163]\n" \
  "ds_read_b64_tr_b16 v[96:97], v74 offset:16384\n" \
  "ds_read_b64_tr_b16 v[98:99], v74 offset:20480\n" \
  "s_waitcnt lgkmcnt(14)\n" \
  "v_mfma_f32_16x16x32_bf16 v[164:167], v[132:135], v[196:199], v[164:167]\n" \
  "ds_read_b64_tr_b16 v[100:101], v75 offset:16384\n" \
  "ds_read_b64_tr_b16 v[102:103], v75 offset:20480\n" \
  "s_waitcnt lgkmcnt(14)\n" \
  "v_mfma_f32_16x16x32_bf16 v[168:171], v[136:139], v[196:199], v[168:171]\n" \
  "ds_read_b64_tr_b16 v[104:105], v76 offset:16384\n" \
  "ds_read_b64_tr_b16 v[106:107], v76 offset:20480\n" \
  "s_waitcnt lgkmcnt(14)\n" \
  "v_mfma_f32_16x16x32_bf16 v[172:175], v[140:143], v[196:199], v[172:175]\n" \
  "ds_read_b64_tr_b16 v[108:109], v77 offset:16384\n" \
  "ds_read_b64_tr_b16 v[110:111], v77 offset:20480\n" \
  "v_cmp_gt_i32 vcc, %[rows], v66\n" \
  "v_mov_b32 v67, 0x7fff0000\n" \
  "v_subrev_u32 v66, 4, v66\n" \
  "s_waitcnt lgkmcnt(14)\n" \
  "v_mfma_f32_16x16x32_bf16 v[144:147], v[80:83], v[200:203], v[144:147]\n" \
  "ds_read_b64_tr_b16 v[112:113], v70 offset:24576\n" \
  "ds_read_b64_tr_b16 v[114:115], v70 offset:28672\n" \
  "s_waitcnt lgkmcnt(14)\n" \
  "v_mfma_f32_16x16x32_bf16 v[148:151], v[84:87], v[200:203], v[148:151]\n" \
  "ds_read_b64_tr_b16 v[116:117], v71 offset:24576\n" \
  "ds_read_b64_tr_b16 v[118:119], v71 offset:28672\n" \
  "s_waitcnt lgkmcnt(14)\n" \
  "v_mfma_f32_16x16x32_bf16 v[152:155], v[88:91], v[200:203], v[152:155]\n" \
  "ds_read_b64_tr_b16 v[120:121], v72 offset:24576\n" \
  "ds_read_b64_tr_b16 v[122:123], v72 offset:28672\n" \
  "s_waitcnt lgkmcnt(14)\n" \
  "v_mfma_f32_16x16x32_bf16 v[156:159], v[92:95], v[200:203], v[156:159]\n" \
  "ds_read_b64_tr_b16 v[124:125], v73 offset:24576\n" \
  "ds_read_b64_tr_b16 v[126:127], v73 offset:28672\n" \
  "s_waitcnt lgkmcnt(14)\n" \
  "v_mfma_f32_16x16x32_bf16 v[160:163], v[96:99], v[200:203], v[160:163]\n" \
  "ds_read_b64_tr_b16 v[128:129], v74 offset:24576\n" \
  "ds_read_b64_tr_b16 v[130:131], v74 offset:28672\n" \
  "s_waitcnt lgkmcnt(14)\n" \
  "v_mfma_f32_16x16x32_bf16 v[164:167], v[100:103], v[200:203], v[164:167]\n" \
  "ds_read_b64_tr_b16 v[132:133], v75 offset:24576\n" \
  "ds_read_b64_tr_b16 v[134:135], v75 offset:28672\n" \
  "s_waitcnt lgkmcnt(14)\n" \
  "v_mfma_f32_16x16x32_bf16 v[168:171], v[104:107], v[200:203], v[168:171]\n" \
  "ds_read_b64_tr_b16 v[136:137], v76 offset:24576\n" \
  "ds_read_b64_tr_b16 v[138:139], v76 offset:28672\n" \
  "s_waitcnt lgkmcnt(14)\n" \
  "v_mfma_f32_16x16x32_bf16 v[172:175], v[108:111], v[200:203], v[172:175]\n" \
  "ds_read_b64_tr_b16 v[140:141], v77 offset:24576\n" \
  "ds_read_b64_tr_b16 v[142:143], v77 offset:28672\n" \
  "v_cndmask_b32 v68, v67, v79, vcc\n" \
  "v_cmp_gt_i32 vcc, %[rows], v66\n" \
  "s_waitcnt lgkmcnt(14)\n" \
  "v_mfma_f32_16x16x32_bf16 v[144:147], v[112:115], v[204:207], v[144:147]\n" \
  "s_waitcnt lgkmcnt(12)\n" \
  "v_mfma_f32_16x16x32_bf16 v[148:151], v[116:119], v[204:207], v[148:151]\n" \
  "s_waitcnt lgkmcnt(10)\n" \
  "v_mfma_f32_16x16x32_bf16 v[152:155], v[120:123], v[204:207], v[152:155]\n" \
  "v_cndmask_b32 v67, v67, v79, vcc\n" \
  "s_nop 2\n" \
  "s_waitcnt lgkmcnt(8)\n" \
  "v_mfma_f32_16x16x32_bf16 v[156:159], v[124:127], v[204:207], v[156:159]\n" \
  "v_cvt_pk_bf16_f32 v208, v144, v145\n" \
  "v_cvt_pk_bf16_f32 v209, v146, v147\n" \
  "ds_write_b64 v69, v[208:209] offset:0\n" \
  "s_waitcnt lgkmcnt(7)\n" \
  "v_mfma_f32_16x16x32_bf16 v[160:163], v[128:131], v[204:207], v[160:163]\n" \
  "v_cvt_pk_bf16_f32 v210, v148, v149\n" \
  "v_cvt_pk_bf16_f32 v211, v150, v151\n" \
  "ds_write_b64 v69, v[210:211] offset:32\n" \
  "s_waitcnt lgkmcnt(6)\n" \
  "v_mfma_f32_16x16x32_bf16 v[164:167], v[132:135], v[204:207], v[164:167]\n" \
  "v_cvt_pk_bf16_f32 v208, v152, v153\n" \
  "v_cvt_pk_bf16_f32 v209, v154, v155\n" \
  "ds_write_b64 v69, v[208:209] offset:64\n" \
  "s_waitcnt lgkmcnt(5)\n" \
  "v_mfma_f32_16x16x32_bf16 v[168:171], v[136:139], v[204:207], v[168:171]\n" \
  "v_cvt_pk_bf16_f32 v210, v156, v157\n" \
  "v_cvt_pk_bf16_f32 v211, v158, v159\n" \
  "ds_write_b64 v69, v[210:211] offset:96\n" \
  "s_waitcnt lgkmcnt(4)\n" \
  "v_mfma_f32_16x16x32_bf16 v[172:175], v[140:143], v[204:207], v[172:175]\n" \
  "v_cvt_pk_bf16_f32 v208, v160, v161\n" \
  "v_cvt_pk_bf16_f32 v209, v162, v163\n" \
  "ds_write_b64 v69, v[208:209] offset:128\n" \
  "v_cvt_pk_bf16_f32 v210, v164, v165\n" \
  "v_cvt_pk_bf16_f32 v211, v166, v167\n" \
  "ds_write_b64 v69, v[210:211] offset:160\n" \
  "s_nop 0\n" \
  "v_cvt_pk_bf16_f32 v208, v168, v169\n" \
  "v_cvt_pk_bf16_f32 v209, v170, v171\n" \
  "ds_write_b64 v69, v[208:209] offset:192\n" \
  "v_cvt_pk_bf16_f32 v210, v172, v173\n" \
  "v_cvt_pk_bf16_f32 v211, v174, v175\n" \
  "ds_write_b64 v69, v[210:211] offset:224\n" \
  "ds_read_b128 v[80:83], v78 offset:0\n" \
  "ds_read_b128 v[84:87], v78 offset:1088\n" \
  "ds_read_b128 v[88:91], v78 offset:2176\n" \
  "ds_read_b128 v[92:95], v78 offset:3264\n" \
  "s_waitcnt lgkmcnt(3)\n" \
  "buffer_store_dwordx4 v[80:83], v79, %[so], 0 offen offset:0 sc1\n" \
  "s_waitcnt lgkmcnt(2)\n" \
  "buffer_store_dwordx4 v[84:87], v79, %[so], 0 offen offset:1024 sc1\n" \
  "s_waitcnt lgkmcnt(1)\n" \
  "buffer_store_dwordx4 v[88:91], v67, %[so], 0 offen offset:2048 sc1\n" \
  "s_waitcnt lgkmcnt(0)\n" \
  "buffer_store_dwordx4 v[92:95], v68, %[so], 0 offen offset:3072 sc1\n" \
  "LZ_%=:\n" \
  "s_waitcnt lgkmcnt(0)\n" \
  "s_nop 4\n"
#define Q_ASM \
  "ds_read_b64 v[192:193], %[x0]\n" \
  "v_xor_b32 v209, 0x20, %[x0]\n" \
  "v_xor_b32 v210, 0x40, %[x0]\n" \
  "v_xor_b32 v211, 0x60, %[x0]\n" \
  "v_xor_b32 v212, 0x80, %[x0]\n" \
  "v_xor_b32 v213, 0xa0, %[x0]\n" \
  "v_xor_b32 v214, 0xc0, %[x0]\n" \
  "v_xor_b32 v215, 0xe0, %[x0]\n" \
  "ds_read_b64 v[194:195], v209\n" \
  "ds_read_b64_tr_b16 v[80:81], v70 offset:16384\n" \
  "ds_read_b64_tr_b16 v[82:83], v70 offset:20480\n" \
  "ds_read_b64_tr_b16 v[84:85], v71 offset:16384\n" \
  "ds_read_b64_tr_b16 v[86:87], v71 offset:20480\n" \
  "ds_read_b64_tr_b16 v[88:89], v72 offset:16384\n" \
  "ds_read_b64_tr_b16 v[90:91], v72 offset:20480\n" \
  "ds_read_b64_tr_b16 v[92:93], v73 offset:16384\n" \
  "ds_read_b64_tr_b16 v[94:95], v73 offset:20480\n" \
  "ds_read_b64 v[196:197], v210\n" \
  "ds_read_b64 v[198:199], v211\n" \
  "ds_read_b64_tr_b16 v[96:97], v74 offset:16384\n" \
  "ds_read_b64_tr_b16 v[98:99], v74 offset:20480\n" \
  "ds_read_b64_tr_b16 v[100:101], v75 offset:16384\n" \
  "ds_read_b64_tr_b16 v[102:103], v75 offset:20480\n" \
  "ds_read_b64_tr_b16 v[104:105], v76 offset:16384\n" \
  "ds_read_b64_tr_b16 v[106:107], v76 offset:20480\n" \
  "ds_read_b64_tr_b16 v[108:109], v77 offset:16384\n" \
  "ds_read_b64_tr_b16 v[110:111], v77 offset:20480\n" \
  "s_waitcnt lgkmcnt(15)\n" \
  "v_mfma_f32_16x16x32_bf16 v[112:115], v[80:83], v[192:195], 0\n" \
  "ds_read_b64_tr_b16 v[144:145], v70 offset:24576\n" \
  "ds_read_b64_tr_b16 v[146:147], v70 offset:28672\n" \
  "s_waitcnt lgkmcnt(15)\n" \
  "v_mfma_f32_16x16x32_bf16 v[116:119], v[84:87], v[192:195], 0\n" \
  "ds_read_b64_tr_b16 v[148:149], v71 offset:24576\n" \
  "ds_read_b64_tr_b16 v[150:151], v71 offset:28672\n" \
  "s_waitcnt lgkmcnt(15)\n" \
  "v_mfma_f32_16x16x32_bf16 v[120:123], v[88:91], v[192:195], 0\n" \
  "ds_read_b64_tr_b16 v[152:153], v72 offset:24576\n" \
  "ds_read_b64_tr_b16 v[154:155], v72 offset:28672\n" \
  "s_waitcnt lgkmcnt(15)\n" \
  "v_mfma_f32_16x16x32_bf16 v[124:127], v[92:95], v[192:195], 0\n" \
  "ds_read_b64_tr_b16 v[156:157], v73 offset:24576\n" \
  "ds_read_b64_tr_b16 v[158:159], v73 offset:28672\n" \
  "ds_read_b64 v[200:201], v212\n" \
  "ds_read_b64 v[202:203], v213\n" \
  "s_waitcnt lgkmcnt(15)\n" \
  "v_mfma_f32_16x16x32_bf16 v[128:131], v[96:99], v[192:195], 0\n" \
  "ds_read_b64_tr_b16 v[160:161], v74 offset:24576\n" \
  "ds_read_b64_tr_b16 v[162:163], v74 offset:28672\n" \
  "s_waitcnt lgkmcnt(15)\n" \
  "v_mfma_f32_16x16x32_bf16 v[132:135], v[100:103], v[192:195], 0\n" \
  "ds_read_b64_tr_b16 v[164:165], v75 offset:24576\n" \
  "ds_read_b64_tr_b16 v[166:167], v75 offset:28672\n" \
  "s_waitcnt lgkmcnt(15)\n" \
  "v_mfma_f32_16x16x32_bf16 v[136:139], v[104:107], v[192:195], 0\n" \
  "ds_read_b64_tr_b16 v[168:169], v76 offset:24576\n" \
  "ds_read_b64_tr_b16 v[170:171], v76 offset:28672\n" \
  "s_waitcnt lgkmcnt(15)\n" \
  "v_mfma_f32_16x16x32_bf16 v[140:143], v[108:111], v[192:195], 0\n" \
  "ds_read_b64_tr_b16 v[172:173], v77 offset:24576\n" \
  "ds_read_b64_tr_b16 v[174:175], v77 offset:28672\n" \
  "s_waitcnt lgkmcnt(15)\n" \
  "v_mfma_f32_16x16x32_bf16 v[112:115], v[144:147], v[196:199], v[112:115]\n" \
  "ds_read_b64_tr_b16 v[80:81], v70 offset:32768\n" \
  "ds_read_b64_tr_b16 v[82:83], v70 offset:36864\n" \
  "s_waitcnt lgkmcnt(15)\n" \
  "v_mfma_f32_16x16x32_bf16 v[116:119], v[148:151], v[196:199], v[116:119]\n" \
  "ds_read_b64_tr_b16 v[84:85], v71 offset:32768\n" \
  "ds_read_b64_tr_b16 v[86:87], v71 offset:36864\n" \
  "s_waitcnt lgkmcnt(15)\n" \
  "v_mfma_f32_16x16x32_bf16 v[120:123], v[152:155], v[196:199], v[120:123]\n" \
  "ds_read_b64_tr_b16 v[88:89], v72 offset:32768\n" \
  "ds_read_b64_tr_b16 v[90:91], v72 offset:36864\n" \
  "s_waitcnt lgkmcnt(15)\n" \
  "v_mfma_f32_16x16x32_bf16 v[124:127], v[156:159], v[196:199], v[124:127]\n" \
  "ds_read_b64_tr_b16 v[92:93], v73 offset:32768\n" \
  "ds_read_b64_tr_b16 v[94:95], v73 offset:36864\n" \
  "ds_read_b64 v[204:205], v214\n" \
  "ds_read_b64 v[206:207], v215\n" \
  "buffer_load_dwordx2 %[gv], %[po], %[sg], 0 offen sc1\n" \
  "s_waitcnt lgkmcnt(15)\n" \
  "v_mfma_f32_16x16x32_bf16 v[128:131], v[160:163], v[196:199], v[128:131]\n" \
  "ds_read_b64_tr_b16 v[96:97], v74 offset:32768\n" \
  "ds_read_b64_tr_b16 v[98:99], v74 offset:36864\n" \
  "s_waitcnt lgkmcnt(15)\n" \
  "v_mfma_f32_16x16x32_bf16 v[132:135], v[164:167], v[196:199], v[132:135]\n" \
  "ds_read_b64_tr_b16 v[100:101], v75 offset:32768\n" \
  "ds_read_b64_tr_b16 v[102:103], v75 offset:36864\n" \
  "s_waitcnt lgkmcnt(15)\n" \
  "v_mfma_f32_16x16x32_bf16 v[136:139], v[168:171], v[196:199], v[136:139]\n" \
  "ds_read_b64_tr_b16 v[104:105], v76 offset:32768\n" \
  "ds_read_b64_tr_b16 v[106:107], v76 offset:36864\n" \
  "s_waitcnt lgkmcnt(15)\n" \
  "v_mfma_f32_16x16x32_bf16 v[140:143], v[172:175], v[196:199], v[140:143]\n" \
  "ds_read_b64_tr_b16 v[108:109], v77 offset:32768\n" \
  "ds_read_b64_tr_b16 v[110:111], v77 offset:36864\n" \
  "s_waitcnt lgkmcnt(15)\n" \
  "v_mfma_f32_16x16x32_bf16 v[112:115], v[80:83], v[200:203], v[112:115]\n" \
  "ds_read_b64_tr_b16 v[144:145], v70 offset:40960\n" \
  "ds_read_b64_tr_b16 v[146:147], v70 offset:45056\n" \
  "s_waitcnt lgkmcnt(15)\n" \
  "v_mfma_f32_16x16x32_bf16 v[116:119], v[84:87], v[200:203], v[116:119]\n" \
  "ds_read_b64_tr_b16 v[148:149], v71 offset:40960\n" \
  "ds_read_b64_tr_b16 v[150:151], v71 offset:45056\n" \
  "s_waitcnt lgkmcnt(15)\n" \
  "v_mfma_f32_16x16x32_bf16 v[120:123], v[88:91], v[200:203], v[120:123]\n" \
  "ds_read_b64_tr_b16 v[152:153], v72 offset:40960\n" \
  "ds_read_b64_tr_b16 v[154:155], v72 offset:45056\n" \
  "s_waitcnt lgkmcnt(15)\n" \
  "v_mfma_f32_16x16x32_bf16 v[124:127], v[92:95], v[200:203], v[124:127]\n" \
  "ds_read_b64_tr_b16 v[156:157], v73 offset:40960\n" \
  "ds_read_b64_tr_b16 v[158:159], v73 offset:45056\n" \
  "s_waitcnt lgkmcnt(14)\n" \
  "v_mfma_f32_16x16x32_bf16 v[128:131], v[96:99], v[200:203], v[128:131]\n" \
  "ds_read_b64_tr_b16 v[160:161], v74 offset:40960\n" \
  "ds_read_b64_tr_b16 v[162:163], v74 offset:45056\n" \
  "s_waitcnt lgkmcnt(14)\n" \
  "v_mfma_f32_16x16x32_bf16 v[132:135], v[100:103], v[200:203], v[132:135]\n" \
  "ds_read_b64_tr_b16 v[164:165], v75 offset:40960\n" \
  "ds_read_b64_tr_b16 v[166:167], v75 offset:45056\n" \
  "s_waitcnt lgkmcnt(14)\n" \
  "v_mfma_f32_16x16x32_bf16 v[136:139], v[104:107], v[200:203], v[136:139]\n" \
  "ds_read_b64_tr_b16 v[168:169], v76 offset:40960\n" \
  "ds_read_b64_tr_b16 v[170:171], v76 offset:45056\n" \
  "s_waitcnt lgkmcnt(14)\n" \
  "v_mfma_f32_16x16x32_bf16 v[140:143], v[108:111], v[200:203], v[140:143]\n" \
  "ds_read_b64_tr_b16 v[172:173], v77 offset:40960\n" \
  "ds_read_b64_tr_b16 v[174:175], v77 offset:45056\n" \
  "s_waitcnt lgkmcnt(14)\n" \
  "v_mfma_f32_16x16x32_bf16 v[112:115], v[144:147], v[204:207], v[112:115]\n" \
  "v_add_u32 v70, 0x20000, v70\n" \
  "v_add_u32 v71, 0x20000, v71\n" \
  "v_add_u32 v72, 0x20000, v72\n" \
  "s_waitcnt lgkmcnt(12)\n" \
  "v_mfma_f32_16x16x32_bf16 v[116:119], v[148:151], v[204:207], v[116:119]\n" \
  "v_add_u32 v73, 0x20000, v73\n" \
  "v_add_u32 v74, 0x20000, v74\n" \
  "v_add_u32 v75, 0x20000, v75\n" \
  "s_waitcnt lgkmcnt(10)\n" \
  "v_mfma_f32_16x16x32_bf16 v[120:123], v[152:155], v[204:207], v[120:123]\n" \
  "v_add_u32 v76, 0x20000, v76\n" \
  "v_add_u32 v77, 0x20000, v77\n" \
  "s_waitcnt lgkmcnt(8)\n" \
  "v_mfma_f32_16x16x32_bf16 v[124:127], v[156:159], v[204:207], v[124:127]\n" \
  "v_cvt_pk_bf16_f32 v32, v112, v113\n" \
  "v_cvt_pk_bf16_f32 v33, v114, v115\n" \
  "s_waitcnt lgkmcnt(6)\n" \
  "v_mfma_f32_16x16x32_bf16 v[128:131], v[160:163], v[204:207], v[128:131]\n" \
  "v_cvt_pk_bf16_f32 v34, v116, v117\n" \
  "v_cvt_pk_bf16_f32 v35, v118, v119\n" \
  "s_waitcnt lgkmcnt(4)\n" \
  "v_mfma_f32_16x16x32_bf16 v[132:135], v[164:167], v[204:207], v[132:135]\n" \
  "v_cvt_pk_bf16_f32 v36, v120, v121\n" \
  "v_cvt_pk_bf16_f32 v37, v122, v123\n" \
  "s_waitcnt lgkmcnt(2)\n" \
  "v_mfma_f32_16x16x32_bf16 v[136:139], v[168:171], v[204:207], v[136:139]\n" \
  "v_cvt_pk_bf16_f32 v38, v124, v125\n" \
  "v_cvt_pk_bf16_f32 v39, v126, v127\n" \
  "s_waitcnt lgkmcnt(0)\n" \
  "v_mfma_f32_16x16x32_bf16 v[140:143], v[172:175], v[204:207], v[140:143]\n" \
  "v_cvt_pk_bf16_f32 v40, v128, v129\n" \
  "v_cvt_pk_bf16_f32 v41, v130, v131\n" \
  "v_permlane32_swap_b32 v32, v34\n" \
  "v_permlane32_swap_b32 v33, v35\n" \
  "v_cvt_pk_bf16_f32 v42, v132, v133\n" \
  "v_cvt_pk_bf16_f32 v43, v134, v135\n" \
  "v_permlane32_swap_b32 v36, v38\n" \
  "v_permlane32_swap_b32 v37, v39\n" \
  "v_permlane16_swap_b32 v32, v34\n" \
  "v_permlane16_swap_b32 v33, v35\n" \
  "v_cvt_pk_bf16_f32 v44, v136, v137\n" \
  "v_cvt_pk_bf16_f32 v45, v138, v139\n" \
  "v_permlane16_swap_b32 v36, v38\n" \
  "v_permlane16_swap_b32 v37, v39\n" \
  "v_cvt_pk_bf16_f32 v46, v140, v141\n" \
  "v_cvt_pk_bf16_f32 v47, v142, v143\n" \
  "v_permlane32_swap_b32 v40, v42\n" \
  "v_permlane32_swap_b32 v41, v43\n" \
  "s_nop 0\n" \
  "v_permlane32_swap_b32 v44, v46\n" \
  "v_permlane32_swap_b32 v45, v47\n" \
  "v_permlane16_swap_b32 v40, v42\n" \
  "v_permlane16_swap_b32 v41, v43\n" \
  "s_nop 0\n" \
  "v_permlane16_swap_b32 v44, v46\n" \
  "v_permlane16_swap_b32 v45, v47\n"
#define ZERO_STATE_ASM \
  "v_mov_b64 v[0:1], 0\nv_mov_b64 v[2:3], 0\nv_mov_b64 v[4:5], 0\nv_mov_b64 v[6:7], 0\n" \
  "v_mov_b64 v[8:9], 0\nv_mov_b64 v[10:11], 0\nv_mov_b64 v[12:13], 0\nv_mov_b64 v[14:15], 0\n" \
  "v_mov_b64 v[16:17], 0\nv_mov_b64 v[18:19], 0\nv_mov_b64 v[20:21], 0\nv_mov_b64 v[22:23], 0\n" \
  "v_mov_b64 v[24:25], 0\nv_mov_b64 v[26:27], 0\nv_mov_b64 v[28:29], 0\nv_mov_b64 v[30:31], 0\n" \
  "v_mov_b64 v[176:177], 0\nv_mov_b64 v[178:179], 0\nv_mov_b64 v[180:181], 0\nv_mov_b64 v[182:183], 0\n" \
  "v_mov_b64 v[184:185], 0\nv_mov_b64 v[186:187], 0\nv_mov_b64 v[188:189], 0\nv_mov_b64 v[190:191], 0\n"

struct State { f32x32 acc; u32x16 q; u32x32 tab; u32x32 prev; u32x16 pz; };
enum { T_ROW = 0, T_TR = 4, T_DMA = 12, T_ID = 16, T_KEY = 18, T_LA, T_LE, T_ST, T_WV, T_RD = T_WV + 8, T_OUT };

DI void issue_rows(const State& S, const Dma& M, unsigned addr, unsigned buf) {
  const u32x4 a = lds_ld<u32x4>(addr), b = lds_ld<u32x4>(addr + 64);
  dma4(M.srd, buf, (a[0] & SRC_MASK) | S.tab[T_DMA], (a[1] & SRC_MASK) | S.tab[T_DMA + 1], (a[2] & SRC_MASK) | S.tab[T_DMA + 2],
       (a[3] & SRC_MASK) | S.tab[T_DMA + 3], M.o0, M.o1, M.o2, M.o3);
  dma4(M.srd, buf + 4096, (b[0] & SRC_MASK) | S.tab[T_DMA], (b[1] & SRC_MASK) | S.tab[T_DMA + 1], (b[2] & SRC_MASK) | S.tab[T_DMA + 2],
       (b[3] & SRC_MASK) | S.tab[T_DMA + 3], M.o0, M.o1, M.o2, M.o3);
}

template <bool zero> DI void request_first(State& S, const Dma& M, unsigned iters, unsigned ring) {
  __builtin_amdgcn_s_setprio(3);
  issue_rows(S, M, S.tab[T_LA], ring);
  __builtin_amdgcn_s_setprio(0);
  if (zero) asm volatile(ZERO_STATE_ASM : "={v[0:31]}"(S.acc), "={v[176:191]}"(S.pz));
  if (iters > 1) issue_rows(S, M, S.tab[T_LA] + 128, ring + 8192);
}

DI void run_list(State& S, const Dma& M, unsigned iters, unsigned ring, unsigned long long more, u32x4 srd_o,
                 int last_rows) {
  u32x32 t0, t1, t2;
  asm volatile(EDGE_LOOP_ASM
               : "+{v[0:31]}"(S.acc), "+{v[48:79]}"(S.tab), "+{v[144:175]}"(S.prev), "+{v[176:191]}"(S.pz),
                 "=&{v[80:111]}"(t0), "=&{v[112:143]}"(t1), "=&{v[192:223]}"(t2), [left] "+s"(iters)
               : "{v[32:47]}"(S.q), [srd] "s"(M.srd), [o0] "s"(M.o0), [o1] "s"(M.o1), [o2] "s"(M.o2), [o3] "s"(M.o3),
                 [ring] "s"(ring), [msk] "s"(SRC_MASK), [more] "s"(more), [so] "s"(srd_o), [rows] "s"(last_rows)
               : "memory", "vcc", "scc");
}

DI void q_product(State& S, u32x8& wa, unsigned x0, unsigned poll_off, u32x4 srd_g, u32x2& gv) {
  u32x32 t0, t1, t2;
  asm volatile(Q_ASM
               : "=&{v[32:47]}"(S.q), "+{v[70:77]}"(wa), "=&{v[80:111]}"(t0), "=&{v[112:143]}"(t1), "=&{v[144:175]}"(S.prev), "=&{v[192:223]}"(t2),
                 [gv] "=&v"(gv)
               : [x0] "v"(x0), [po] "v"(poll_off), [sg] "s"(srd_g)
               : "memory");
}

DI void touch(u32x4 srd, unsigned off) {
  asm volatile("s_mov_b32 m0, %2\n\ts_nop 0\n\tbuffer_load_dword %0, %1, 0 offen lds" :: "v"(off), "s"(srd), "s"(L_JUNK) : "memory");
}
DI bool poll(const char* ws, unsigned index, unsigned tag, unsigned tag2, unsigned& lo) {
  GLB_AS unsigned long long* p = (GLB_AS unsigned long long*)(ws + W_GRAN) + index;
  unsigned long long gv;
  unsigned spins = 0;
  bool ok;
  do {
    gv = __hip_atomic_load(p, __ATOMIC_RELAXED, __HIP_MEMORY_SCOPE_AGENT);
    ok = (unsigned)(gv >> 32) == tag && ((unsigned)gv ^ tag2) < (1u << 24);
  } while (!ok && ++spins < (1u << 20));
  lo = (unsigned)gv;
  return ok;
}

DI void helper_wave(const char* x, const char* wq, const char* wv, char* ws, unsigned N, unsigned c, unsigned bucket,
                    unsigned lane) {
  unsigned raw;
  asm volatile("global_load_dword %0, %1, %2" : "=v"(raw) : "v"(0u), "s"(ws) : "memory");
  touch(make_srd((c & 8) ? wv : wq, D * D * 2), lane < 16 ? ((c >> 4) * 16 + lane) * 128 : OOB);
  const unsigned g = lane >> 4, c16 = lane & 15;
  unsigned dm[4];
#pragma unroll
  for (int q = 0; q < 4; ++q) dm[q] = g * 256 + ((c16 ^ swz(4 * q + g)) << 4);
  const u32x4 srd_x = make_srd(x, N * 256u);
  unsigned off = ((c & 7) + 1) * GROUP_ROWS * 256 + (c >> 3) * 8192 + lane * 128;
  auto warm = [&](int n) {
    for (int i = 0; i < n; ++i) { touch(srd_x, min(off, off - N * 256)); off += 32 * 8192; }
  };
  auto stage = [&](const char* wm, unsigned dstb) {
    const u32x4 srd = make_srd(wm, D * D * 2);
#pragma unroll
    for (unsigned o = 0; o < D * D * 2; o += 4096) dma4(srd, dstb + o, dm[0], dm[1], dm[2], dm[3], o, o, o, o);
  };
  bar();
  __builtin_amdgcn_s_sleep(XO_SLEEP);
  {
    const unsigned row = bucket * (BN * 256);
#pragma unroll
    for (unsigned t = 0; t < NW; ++t) {
      const unsigned o = row + tile_start(t) * 256;
      dma4(srd_x, L_XO + t * 4096, dm[0], dm[1], dm[2], dm[3], o, o, o, o);
    }
  }
  bar();
  bar();
  __builtin_amdgcn_s_sleep(WQ_SLEEP);
  stage(wq, L_WQ);
  bar();
  asm volatile("s_waitcnt vmcnt(0)" : "+v"(raw) :: "memory");
  const unsigned tag = __builtin_amdgcn_readfirstlane(raw) + TAG_STEP, tag2 = (tag * 167u) & 0xff000000u;
  if (c >= NC && lane == 0)
    __hip_atomic_store((GLB_AS unsigned long long*)(ws + W_GRAN) + NC * NB + c, ((unsigned long long)tag << 32) | tag2,
                       __ATOMIC_RELAXED, __HIP_MEMORY_SCOPE_AGENT);
  bar();
  stage(wv, L_WV);
  warm(PF2);
  wait_vm<PF2>();
#pragma unroll 1
  for (int i = 0; i < PF3; ++i) { wait_vm<PFD>(); warm(1); }
  bar();
  if (c == 0) {
    unsigned lo;
    poll(ws, NC * NB + NC + (lane & (NB - NC - 1)), tag, tag2, lo);
    if (lane == 0) __hip_atomic_store((GLB_AS unsigned*)ws, tag, __ATOMIC_RELAXED, __HIP_MEMORY_SCOPE_AGENT);
  }
  wait_vm<0>();
}

__global__ __launch_bounds__(512, 2) void graph_kernel(const char*, const char*, const char*, const char*, const char*, char*, char*,
                                                       unsigned, unsigned, unsigned, unsigned) {
  __shared__ __attribute__((aligned(16))) char smem[L_TOTAL];
  asm volatile("" :: "v"((LDS_AS char*)smem));
  u32x16 args;
  u32x2 args2;
  asm volatile("s_load_dwordx16 %0, %2, 0x0\n\ts_load_dwordx2 %1, %2, 0x40"
               : "=&{s[36:51]}"(args), "=&{s[52:53]}"(args2) : "s"(__builtin_amdgcn_kernarg_segment_ptr()) : "memory");
  {
    unsigned off;
    asm volatile("s_cmp_ge_u32 %3, %4\n\ts_cbranch_scc1 LC%=\n\t"
                 "s_getpc_b64 s[92:93]\n\tv_lshlrev_b32 %0, 7, %2\n\ts_mov_b64 s[94:95], %1\n\ts_movk_i32 m0, %5\n\t"
                 "s_and_b32 s93, s93, 0xffff\n\tbuffer_load_dword %0, s[92:95], 0 offen lds\nLC%=:"
                 : "=&v"(off) : "s"(0x0002000000000000ull | CODE_BYTES), "v"(threadIdx.x), "s"(blockIdx.x), "i"(CODE_WGS), "i"(L_JUNK)
                 : "memory", "scc", "s92", "s93", "s94", "s95");
  }
  SCHED_FENCE();
  const unsigned tid = threadIdx.x, lane = tid & 63;
  const unsigned w = __builtin_amdgcn_readfirstlane(tid >> 6);
  {
    const unsigned k = tid & 127, t = tile_of(k);
    const unsigned d = (k - tile_start(t)) & 15;
    const unsigned node = (d << 4) | ((d >> 2) << 24) | (t << 27);
    unsigned cursor = L_LIST + (lane >> 1) * TILE_BYTES + LO_CAP * 4 - (lane & 1) * 4;
    pin(cursor);
    cursor = lane < 2 * NW ? cursor : 0;
    unsigned v = tid < NB ? 0 : node, a = L_HIST + tid * 4;
    pin(v); pin(a);
    lds_st<unsigned>(w < 6 ? a : w == 6 ? L_CNT + lane * 4 : L_JUNK, w < 6 ? v : cursor);
  }
  SCHED_FENCE();
  asm volatile("s_waitcnt lgkmcnt(0)" : "+{s[36:51]}"(args), "+{s[52:53]}"(args2) :: "memory");
  auto pointer = [&](int i) { return (char*)(((unsigned long long)args[2 * i + 1] << 32) | args[2 * i]); };
  const char *x = pointer(0), *src = pointer(1), *dst = pointer(2), *wq = pointer(3), *wv = pointer(4);
  char *out = pointer(5), *ws = pointer(6);
  const unsigned N = args[14], E = args[15], chunk = args2[0];
  const unsigned c = blockIdx.x;
  const unsigned bucket = (c & 7) * (NB / 8) + (c >> 3);
  if (w == NW) {
    helper_wave(x, wq, wv, ws, N, c, bucket, lane);
    return;
  }

  const unsigned e0 = c * chunk + 4 * tid;
  const bool have = 4 * tid < chunk && e0 < E;
  const unsigned eoff = have ? e0 * 4 : OOB;
  u32x4 d4, s4;
  unsigned epoch;
  asm volatile("buffer_load_dwordx4 %0, %3, %4, 0 offen\n\t"
               "buffer_load_dwordx4 %1, %3, %5, 0 offen\n\t"
               "global_load_dword %2, %6, %7"
               : "=&v"(d4), "=&v"(s4), "=&v"(epoch)
               : "v"(eoff), "s"(make_srd(dst, E * 4)), "s"(make_srd(src, E * 4)), "v"(0u), "s"(ws) : "memory");
  SCHED_FENCE();

  const unsigned g = lane >> 4, c16 = lane & 15, tq = c16 >> 2, tp = c16 & 3;
  Dma M;
  M.srd = make_srd(x - 3072, N * 256u + 3072u);
  M.o0 = 3072; M.o1 = 2048; M.o2 = 1024; M.o3 = 0;
  u32x4 srd_o = make_srd(out, N * 256u);
  u32x4 srd_e = make_srd(ws + W_ENT, (NC * chunk + 64) * 4u), srd_g = make_srd(ws + W_GRAN, NC * NB * 8);
  asm volatile("" : "+s"(M.srd), "+s"(M.o0), "+s"(M.o1), "+s"(M.o2), "+s"(M.o3), "+s"(srd_o), "+s"(srd_e), "+s"(srd_g));
  unsigned dm[4];
#pragma unroll
  for (int q = 0; q < 4; ++q) { dm[q] = (c16 ^ swz(4 * q + g)) << 4; pin(dm[q]); }
  unsigned r0 = c16 * 256 + ((g ^ swz(c16)) << 4);
  unsigned t0 = (4 * g + tq) * 256 + (((tp >> 1) ^ swz(4 * g + tq)) << 4) + 8 * (tp & 1);
  unsigned x0 = L_XO + w * 4096 + c16 * 256 + (((g >> 1) ^ swz(c16)) << 4) + 8 * (g & 1);
  pin(r0); pin(t0); pin(x0);
  bar();

  asm volatile("s_waitcnt vmcnt(0)" : "+v"(d4), "+v"(s4), "+v"(epoch) :: "memory");
  unsigned hb[4], rank[4] = {0, 0, 0, 0}, ent[4];
  if (E & 3) {
#pragma unroll
    for (int j = 1; j < 4; ++j) s4[j] = e0 + j < E ? s4[j] : N;
  }
#pragma unroll
  for (int j = 0; j < 4; ++j) hb[j] = (__umul24(d4[j], 21400) >> 21) * 4;
  if (have) {
#pragma unroll
    for (int j = 0; j < 4; ++j) rank[j] = lds_add(L_HIST + hb[j], 4);
  }
  unsigned lut[4];
#pragma unroll
  for (int j = 0; j < 4; ++j) lut[j] = lds_ld<unsigned>(L_LUT + mad24(hb[j], -BN, d4[j] * 4));
  SCHED_FENCE();
#pragma unroll
  for (int j = 0; j < 4; ++j) {
    const unsigned rel = mad24(hb[j] >> 7, -(int)(GROUP_ROWS), s4[j]);
    ent[j] = (min(rel, rel + N) < WARM_ROWS ? (1u << 26) | 0xC : 0x4) | (s4[j] << 8);
    pin(ent[j]);
  }
  bar();
  {
    const u32x4 h = lds_ld<u32x4>(L_HIST + 16 * lane);
    const unsigned tot = h[0] + h[1] + h[2] + h[3];
    unsigned inc = tot;
    inc = dpp_add<0x111>(inc);
    inc = dpp_add<0x112>(inc);
    inc = dpp_add<0x114>(inc);
    inc = dpp_add<0x118>(inc);
    inc = dpp_add<0x142, 0xa>(inc);
    inc = dpp_add<0x143, 0xc>(inc);
    const unsigned ex = inc - tot;
    lds_st<u32x4>(L_START + 16 * lane, u32x4{ex, ex + h[0], ex + h[0] + h[1], ex + h[0] + h[1] + h[2]});
    unsigned pos[4];
#pragma unroll
    for (int j = 0; j < 4; ++j) pos[j] = lds_ld<unsigned>(L_START + hb[j]) + rank[j];
    if (have) {
#pragma unroll
      for (int j = 0; j < 4; ++j) lds_st<unsigned>(L_SORT + pos[j], ent[j] | lut[j]);
    }
  }
  bar();
  {
    const u32x4 v = lds_ld<u32x4>(L_SORT + 16 * tid);
    asm volatile("buffer_store_dwordx4 %0, %1, %2, 0 offen sc1\n\ts_nop 1" :: "v"(v), "v"(eoff), "s"(srd_e) : "memory");
  }
  const unsigned tag = __builtin_amdgcn_readfirstlane(epoch) + TAG_STEP, tag2 = (tag * 167u) & 0xff000000u;
  u32x2 gran = {(lds_ld<unsigned>(L_START + (tid & 255) * 4) >> 2) | (lds_ld<unsigned>(L_HIST + (tid & 255) * 4) << 10) | tag2, tag};
  unsigned goff = tid < NB ? (c * NB + tid) * 8 : OOB;
  pin(gran); pin(goff);
  State S;
  u32x8 wa;
  const unsigned seg = tid >> 1, sj = tid & 1;
  unsigned seg_base = seg * chunk + 4 * sj;
  unsigned poll_off = (seg * NB + bucket) * 8;
  pin(seg_base); pin(poll_off);
#pragma unroll
  for (int t = 0; t < 8; ++t) wa[t] = t0 ^ (t << 5);
  asm volatile("" : "+{v[70:77]}"(wa));
  wait_vm<0>();
  bar();
  asm volatile("buffer_store_dwordx2 %0, %1, %2, 0 offen sc1" :: "v"(gran), "v"(goff), "s"(srd_g) : "memory");
  bar();

  u32x2 gv;
  q_product(S, wa, x0, poll_off, srd_g, gv);
  asm volatile("s_waitcnt vmcnt(0)" : "+v"(gv) :: "memory");
  unsigned lo = gv[0];
  bool ok = gv[1] == tag && (lo ^ tag2) < (1u << 24);
  if (!ok) ok = poll(ws, poll_off >> 3, tag, tag2, lo);
  const unsigned seg_cnt = ok ? (lo >> 12) & 0xfff : 0;
  const unsigned seg_off = (seg_base + (lo & 0xfff)) * 4;
  const int rem = (int)seg_cnt - 4 * (int)sj;
  const unsigned long long over = __builtin_amdgcn_ballot_w64(rem > 16);
  const bool third = over != 0;
  u32x4 ea, eb, ec;
  {
    const unsigned va = rem > 0 ? seg_off : OOB, vb = rem > 8 ? seg_off + 32 : OOB, vc = rem > 16 ? seg_off + 64 : OOB;
    asm volatile("buffer_load_dwordx4 %0, %3, %6, 0 offen sc1\n\t"
                 "buffer_load_dwordx4 %1, %4, %6, 0 offen sc1\n\t"
                 "s_cmp_eq_u64 %7, 0\n\t"
                 "s_cbranch_scc1 LS%=\n\t"
                 "buffer_load_dwordx4 %2, %5, %6, 0 offen sc1\n"
                 "LS%=:"
                 : "=&v"(ea), "=&v"(eb), "=&v"(ec) : "v"(va), "v"(vb), "v"(vc), "s"(srd_e), "s"(over) : "memory", "scc");
  }
  const unsigned ring = L_RING + w * 16384;
#pragma unroll
  for (int s = 0; s < 4; ++s) { S.tab[T_ROW + s] = (r0 + ring) ^ (s << 6); S.tab[T_DMA + s] = dm[s]; }
#pragma unroll
  for (int t = 0; t < 8; ++t) { S.tab[T_TR + t] = (t0 + ring) ^ (t << 5); S.tab[T_WV + t] = wa[t]; }
  S.tab[T_ID] = tq == g && tp < 2 ? 0x3F80u << (16 * tp) : 0;
  S.tab[T_ID + 1] = tq == g && tp >= 2 ? 0x3F80u << (16 * (tp & 1)) : 0;
  S.tab[T_KEY] = g;
  S.tab[T_LA] = 16 * g;
  S.tab[T_LE] = 16 * tp + 4 * tq;
  S.tab[T_ST] = ring + c16 * 272 + 8 * g;
  S.tab[T_RD] = ring + g * 272 + 16 * c16;
  S.tab[T_OUT] = (bucket * BN + tile_start(w) + g) * 256 + 16 * c16;
  asm volatile("" : "+{v[48:79]}"(S.tab));
  unsigned cur_lo = L_LIST + ((lane & 15) >> 1) * TILE_BYTES, cur_span = TILE_BYTES - 32 * 4;
  if ((lane & 15) >= 2 * NW) { cur_lo = 0; cur_span = 0; }
  unsigned cur_own = L_CNT + 8 * w, cur_all = L_CNT + (lane & 15) * 4, pad_off = lane < 60 ? lane * 4 : lane * 4 - 256;
  pin(cur_lo); pin(cur_span); pin(cur_own); pin(cur_all); pin(pad_off);
  asm volatile("s_waitcnt vmcnt(0)" : "+v"(ea), "+v"(eb), "+v"(ec) :: "memory");
  unsigned e8[8] = {ea[0], ea[1], ea[2], ea[3], eb[0], eb[1], eb[2], eb[3]};
  {
    unsigned a[8], st[8];
#define CUR_OF(D, S) "v_and_b32_sdwa " D ", %[m], " S " dst_sel:DWORD dst_unused:UNUSED_PAD src0_sel:DWORD src1_sel:BYTE_3\n\t"
#define STEP_OF(D, S) "v_bfe_i32 " D ", " S ", 0, 4\n\t"
#define PREP(A, T, S) CUR_OF(A, S) STEP_OF(T, S)
#define TAKE(K, A, T) "v_cmpx_lt_i32 vcc, " K ", %[r]\n\tds_add_rtn_u32 " A ", " A ", " T "\n\t"
#define STRIP(S) "v_and_b32 " S ", %[k], " S "\n\t"
#define PUT(A, T) "v_med3_u32 " A ", " A ", %[lo], %[hi]\n\tds_write_b32 " A ", " T "\n\t"
    asm volatile(PREP("%0", "%8", "%16") PREP("%1", "%9", "%17") PREP("%2", "%10", "%18") PREP("%3", "%11", "%19")
                 PREP("%4", "%12", "%20") PREP("%5", "%13", "%21") PREP("%6", "%14", "%22") PREP("%7", "%15", "%23")
                 TAKE("0", "%0", "%8") TAKE("1", "%1", "%9") TAKE("2", "%2", "%10") TAKE("3", "%3", "%11")
                 TAKE("8", "%4", "%12") TAKE("9", "%5", "%13") TAKE("10", "%6", "%14") TAKE("11", "%7", "%15")
                 "s_mov_b64 exec, -1\n\t"
                 STRIP("%16") STRIP("%17") STRIP("%18") STRIP("%19") STRIP("%20") STRIP("%21") STRIP("%22") STRIP("%23")
                 "s_waitcnt lgkmcnt(0)\n\t"
                 PUT("%0", "%16") PUT("%1", "%17") PUT("%2", "%18") PUT("%3", "%19")
                 PUT("%4", "%20") PUT("%5", "%21") PUT("%6", "%22") PUT("%7", "%23")
                 : "=&v"(a[0]), "=&v"(a[1]), "=&v"(a[2]), "=&v"(a[3]), "=&v"(a[4]), "=&v"(a[5]), "=&v"(a[6]), "=&v"(a[7]),
                   "=&v"(st[0]), "=&v"(st[1]), "=&v"(st[2]), "=&v"(st[3]), "=&v"(st[4]), "=&v"(st[5]), "=&v"(st[6]), "=&v"(st[7]),
                   "+v"(e8[0]), "+v"(e8[1]), "+v"(e8[2]), "+v"(e8[3]), "+v"(e8[4]), "+v"(e8[5]), "+v"(e8[6]), "+v"(e8[7])
                 : [r] "v"(rem), [m] "s"(0x3cu), [k] "s"(LIST_MASK), [lo] "v"(L_LIST - 4), [hi] "s"(L_RING - 4)
                 : "memory", "vcc");
    if (__builtin_expect(third, 0)) {
#pragma unroll 1
      for (int k = 16;;) {
        unsigned e4[4] = {ec[0], ec[1], ec[2], ec[3]};
        asm volatile(PREP("%0", "%4", "%8") PREP("%1", "%5", "%9") PREP("%2", "%6", "%10") PREP("%3", "%7", "%11")
                     TAKE("0", "%0", "%4") TAKE("1", "%1", "%5") TAKE("2", "%2", "%6") TAKE("3", "%3", "%7")
                     "s_mov_b64 exec, -1\n\t"
                     STRIP("%8") STRIP("%9") STRIP("%10") STRIP("%11")
                     "s_waitcnt lgkmcnt(0)\n\t"
                     PUT("%0", "%8") PUT("%1", "%9") PUT("%2", "%10") PUT("%3", "%11")
                     : "=&v"(a[0]), "=&v"(a[1]), "=&v"(a[2]), "=&v"(a[3]), "=&v"(st[0]), "=&v"(st[1]), "=&v"(st[2]), "=&v"(st[3]),
                       "+v"(e4[0]), "+v"(e4[1]), "+v"(e4[2]), "+v"(e4[3])
                     : [r] "v"(rem - k), [m] "s"(0x3cu), [k] "s"(LIST_MASK), [lo] "v"(L_LIST - 4), [hi] "s"(L_RING - 4)
                     : "memory", "vcc");
        k += 8;
        if (k >= SEG_FAST || __builtin_amdgcn_ballot_w64(rem > k) == 0) break;
        const unsigned vc = rem > k ? seg_off + 4 * k : OOB;
        asm volatile("buffer_load_dwordx4 %0, %1, %2, 0 offen sc1\n\ts_waitcnt vmcnt(0)" : "=&v"(ec) : "v"(vc), "s"(srd_e) : "memory");
      }
      if (seg_cnt > SEG_FAST) lds_st<unsigned>(L_CNT + 15 * 4, 4);
    }
#undef CUR_OF
#undef STEP_OF
#undef PREP
#undef TAKE
#undef STRIP
#undef PUT
  }
  bar();
  unsigned long long slow_mask;
  unsigned tiles, la = S.tab[T_LA], le = S.tab[T_LE];
  {
    u32x2 cw;
    unsigned t, u;
    asm volatile("ds_read_b64 %[cw], %[ca]\n\t"
                 "ds_read_b32 %[t], %[cb]\n\t"
                 "s_waitcnt lgkmcnt(0)"
                 : [cw] "=&v"(cw), [t] "=&v"(t) : [ca] "v"(cur_own), [cb] "v"(cur_all) : "memory");
    unsigned last = cw[0], first = cw[1];
    asm volatile("v_sub_u32 %[t], %[t], %[lo]\n\t"
                 "v_cmp_gt_u32 vcc, %[t], %[span]\n\t"
                 "v_add_u32 %[first], 4, %[first]\n\t"
                 "s_mov_b64 %[slow], vcc\n\t"
                 "s_cbranch_vccz LF%=\n\t"
                 "v_mov_b32 %[first], %[mid]\n\t"
                 "v_mov_b32 %[last], %[mid]\n"
                 "LF%=:\n\t"
                 "v_cndmask_b32 %[t], %[first], %[last], %[up]\n\t"
                 "v_and_b32 %[first], -16, %[first]\n\t"
                 "v_add_u32 %[t], %[t], %[pad]\n\t"
                 "v_sub_u32 %[last], %[last], %[first]\n\t"
                 "v_sub_u32 %[u], %[t], %[first]\n\t"
                 "v_add_u32 %[last], 0x7f, %[last]\n\t"
                 "v_add_u32 %[la], %[la], %[first]\n\t"
                 "v_max_u32 %[last], 0x80, %[last]\n\t"
                 "v_add_u32 %[le], %[le], %[first]\n\t"
                 "v_and_b32 %[last], 0xffffff80, %[last]\n\t"
                 "v_cmpx_lt_u32 vcc, %[u], %[last]\n\t"
                 "ds_write_b32 %[t], %[sent]\n\t"
                 "s_mov_b64 exec, -1\n\t"
                 "v_lshrrev_b32 %[last], 7, %[last]"
                 : [t] "+v"(t), [u] "=&v"(u), [first] "+v"(first), [last] "+v"(last), [la] "+v"(la), [le] "+v"(le), [slow] "=&s"(slow_mask)
                 : [lo] "v"(cur_lo), [span] "v"(cur_span), [mid] "s"(L_LIST + w * TILE_BYTES + LO_CAP * 4),
                   [up] "s"(0x0FFFFFFFFFFFFFFFull), [pad] "v"(pad_off), [sent] "v"(SENTINEL)
                 : "memory", "vcc");
    tiles = last;
  }
  const bool slow = slow_mask != 0;
  const int last_rows = (int)(tile_start(w + 1) - tile_start(w)) - 12;
  {
    const unsigned iters = __builtin_amdgcn_readfirstlane(tiles);
    S.tab[T_LA] = la;
    S.tab[T_LE] = le;
    request_first<true>(S, M, iters, ring);
    run_list(S, M, iters, ring, slow_mask, srd_o, last_rows);
  }

  if (__builtin_expect(slow, 0)) for (unsigned base = 0;; base += 8) {
    bar();
    if (tid < 8) lds_st<unsigned>(L_CNT + tid * 4, 0);
    const int left = rem - (int)base;
    const unsigned va = left > 0 ? seg_off + base * 4 : OOB;
    asm volatile("buffer_load_dwordx4 %0, %1, %2, 0 offen sc1\n\ts_waitcnt vmcnt(0)" : "=&v"(ea) : "v"(va), "s"(srd_e) : "memory");
    bar();
    unsigned r4[4];
#pragma unroll
    for (int j = 0; j < 4; ++j)
      if (j < left) r4[j] = lds_add(L_CNT + ((ea[j] >> 27) & 7) * 4, 1);
    if (seg_cnt > base + 8) lds_st<unsigned>(L_CNT + 28, 1);
    bar();
    const unsigned cn = lds_ld<unsigned>(L_CNT + (lane & 7) * 4);
    const unsigned more = __builtin_amdgcn_readlane(cn, 7);
    const unsigned pad = (lane & 7) < NW ? max((cn + 31) & ~31u, 32u) : 0;
    unsigned sc = lane < 8 ? pad : 0;
    sc = dpp_add<0x111>(sc);
    sc = dpp_add<0x112>(sc);
    sc = dpp_add<0x114>(sc);
    const unsigned st = sc - pad;
#pragma unroll
    for (int j = 0; j < 4; ++j) {
      const unsigned s = __builtin_amdgcn_ds_bpermute(((ea[j] >> 27) & 7) * 4, st);
      if (j < left) lds_st<unsigned>(L_LIST + (s + r4[j]) * 4, ea[j] & LIST_MASK);
    }
    const unsigned n = __builtin_amdgcn_readlane(cn, w), iters = max((n + 31) >> 5, 1u);
    const unsigned lst = L_LIST + __builtin_amdgcn_readlane(st, w) * 4;
    if (n + lane < iters * 32) lds_st<unsigned>(lst + (n + lane) * 4, SENTINEL);
    bar();
    S.tab[T_LA] = lst + 16 * g;
    S.tab[T_LE] = lst + 16 * tp + 4 * tq;
    S.pz = 0;
    request_first<false>(S, M, iters, ring);
    run_list(S, M, iters, ring, more, srd_o, last_rows);
    if (!more) break;
  }
}

}

extern "C" void kernel_launch(void* const* d_in, const int* in_sizes, int n_in, void* d_out, int out_size,
                              void* d_ws, size_t ws_size, hipStream_t stream) {
  const unsigned N = in_sizes[0] / D, E = in_sizes[1];
  const unsigned chunk = ((E + NC - 1) / NC + 3) & ~3u;
  graph_kernel<<<NB, 512, 0, stream>>>((const char*)d_in[0], (const char*)d_in[1], (const char*)d_in[2],
                                       (const char*)d_in[3], (const char*)d_in[4], (char*)d_out, (char*)d_ws,
                                       N, E, chunk, 0u);
}
```
